```python
import torch

def _softmax(x, axis=-1):
    return torch.softmax(x, dim=axis)
def _gelu(x):
    return torch.nn.functional.gelu(x, approximate="tanh")
def _silu(x):
    return torch.nn.functional.silu(x)
def _sigmoid(x):
    return torch.sigmoid(x)
def _layernorm(x, axis=-1, eps=1e-5):
    m = x.mean(dim=axis, keepdim=True)
    v = x.var(dim=axis, keepdim=True, unbiased=False)
    return (x - m) / torch.sqrt(v + eps)
def _scatter_add(tgt, idx, src, axis=0):
    return tgt.clone().index_add_(axis, idx, src)
def _segment_sum(src, seg, n):
    out = torch.zeros((n,) + src.shape[1:], dtype=src.dtype)
    return out.index_add_(0, seg, src)
def _pad(x, pw):
    flat = tuple(v for pair in reversed(pw) for v in pair)
    return torch.nn.functional.pad(x, flat)

kernel_name = 'sequential_preLN__mha_block__dense_activation_squared_relu__prompt'

def setup_inputs():

    _w = (lambda a: (lambda t: t.to(torch.bfloat16) if t.is_floating_point() else t)(torch.as_tensor(a)))
    def _A_setup():
        B, S, D, H = 1, 2048, 640, 32
        return {
            "x": _w((lambda *s: torch.randn(*s, dtype=torch.bfloat16) * 0.05)(B, S, D)),
            "wqkv": _w((lambda *s: torch.randn(*s, dtype=torch.bfloat16) * 0.05)(D, 3 * D)),
            "wo": _w((lambda *s: torch.randn(*s, dtype=torch.bfloat16) * 0.05)(D, D)),
        }
    _A_in = _A_setup()

    def _B_setup():
        B, S, D, F = 1, 2048, 640, 1536
        return {
            "x": _w((lambda *s: torch.randn(*s, dtype=torch.bfloat16) * 0.05)(B, S, D)),
            "w1": _w((lambda *s: torch.randn(*s, dtype=torch.bfloat16) * 0.05)(D, F)),
            "b1": _w((lambda *s: torch.randn(*s, dtype=torch.bfloat16) * 0.8)(F)),
            "w2": _w((lambda *s: torch.randn(*s, dtype=torch.bfloat16) * 0.05)(F, D)),
            "b2": _w((lambda *s: torch.randn(*s, dtype=torch.bfloat16) * 3.2)(D)),
        }
    _B_in = _B_setup()

    return {
        "A_x": _A_in["x"],
        "A_wqkv": _A_in["wqkv"],
        "A_wo": _A_in["wo"],
        "B_x": _B_in["x"],
        "B_w1": _B_in["w1"],
        "B_b1": _B_in["b1"],
        "B_w2": _B_in["w2"],
        "B_b2": _B_in["b2"],
    }

def reference(A_x, A_wqkv, A_wo, B_x, B_w1, B_b1, B_w2, B_b2):
    def _A_ref(x, wqkv, wo):
        B, S, D = x.shape
        H = 32
        Dh = D // H
        qkv = (lambda x, *d: x.permute(*d))((x @ wqkv).reshape(B, S, 3, H, Dh), 2, 0, 3, 1, 4)
        q, k, v = qkv[0], qkv[1], qkv[2]
        scores = torch.einsum("bhqd,bhkd->bhqk", q, k) * (Dh ** -0.5)
        p = _softmax(scores, axis=-1)
        o = torch.einsum("bhqk,bhkd->bhqd", p, v)
        return (lambda x, *d: x.permute(*d))(o, 0, 2, 1, 3).reshape(B, S, D) @ wo

    def _B_ref(x, w1, b1, w2, b2):
        h = torch.einsum("bsd,df->bsf", x, w1) + b1
        zero = torch.zeros_like(h)
        r = torch.where(h > zero, h, zero)
        h = r * r
        out = torch.einsum("bsf,fd->bsd", h, w2) + b2
        return out

    x = A_x
    h = x + _A_ref(_layernorm(x, axis=-1), A_wqkv, A_wo)
    h2 = h + _B_ref(_layernorm(h, axis=-1), B_w1, B_b1, B_w2, B_b2)
    return h2
```

```cpp
#include <hip/hip_runtime.h>
#include <hip/hip_bf16.h>
#include <stdint.h>

#define LAS __attribute__((address_space(3)))
#define GAS __attribute__((address_space(1)))
typedef __bf16 bf16;
typedef __bf16 bf16x2 __attribute__((ext_vector_type(2)));
typedef __bf16 bf16x4 __attribute__((ext_vector_type(4)));
typedef __bf16 bf16x8 __attribute__((ext_vector_type(8)));
typedef short s16x4 __attribute__((ext_vector_type(4)));
typedef float f32x2 __attribute__((ext_vector_type(2)));
typedef float f32x4 __attribute__((ext_vector_type(4)));
typedef float f32x16 __attribute__((ext_vector_type(16)));
typedef unsigned u32x2 __attribute__((ext_vector_type(2)));
typedef unsigned u32x4 __attribute__((ext_vector_type(4)));

constexpr int S = 2048, D = 640, NH = 32, DH = 20, FF = 1536;
constexpr int KV_TILE = 2880;
constexpr int KV_HEAD = 64 * KV_TILE;
constexpr size_t WS_Q = 0;
constexpr size_t WS_KV = WS_Q + (size_t)NH * S * 48;
constexpr size_t WS_O = WS_KV + (size_t)NH * KV_HEAD;
constexpr size_t WS_H = WS_O + (size_t)S * D * 2;
constexpr size_t WS_G = WS_H + (size_t)S * D * 2;
constexpr size_t WS_WO = WS_G + (size_t)S * FF * 2;
constexpr size_t WS_W1 = WS_WO + (size_t)D * D * 2;
constexpr size_t WS_W2 = WS_W1 + (size_t)D * FF * 2;

template <int N> __device__ __forceinline__ void wait_vm() { asm volatile("s_waitcnt vmcnt(%0)" ::"n"(N) : "memory"); }
__device__ __forceinline__ void bar() { asm volatile("s_waitcnt lgkmcnt(0)" ::: "memory"); __builtin_amdgcn_s_barrier(); asm volatile("" ::: "memory"); }
__device__ __forceinline__ void glds16(const void* g, LAS char* l) {
  __builtin_amdgcn_global_load_lds((const GAS void*)g, (LAS void*)l, 16, 0, 0);
}
__device__ __forceinline__ unsigned pk2(float a, float b) {
  f32x2 f = {a, b};
  return __builtin_bit_cast(unsigned, __builtin_convertvector(f, bf16x2));
}
__device__ __forceinline__ float bflo(unsigned u) { return __builtin_bit_cast(float, u << 16); }
__device__ __forceinline__ float bfhi(unsigned u) { return __builtin_bit_cast(float, u & 0xffff0000u); }
__device__ __forceinline__ float dot2(unsigned a, unsigned b, float c) {
  return __builtin_amdgcn_fdot2_f32_bf16(__builtin_bit_cast(bf16x2, a), __builtin_bit_cast(bf16x2, b), c, false);
}

enum { EPI_QKV = 0, EPI_WO = 1, EPI_FF1 = 2, EPI_FF2 = 3 };

struct GemmArgs {
  const bf16* A;
  const bf16* B;
  const bf16* res;
  const bf16* bias;
  char* out;
};

template <int BM, int BN, int KTOT, int KS, int WM, int WN, int WK, int NS, bool RAWB, bool STATS, int EPI, bool SWAP>
__device__ __forceinline__ void gemm_body(const GemmArgs& a, int rb, int cb, int rot, LAS char* lds) {
  constexpr int MT = BM / WM / 16, NT = BN / WN / 16;
  constexpr int NKT = KTOT / (64 * KS);
  constexpr int A_SUB = BM * 128, B_SUB = BN * 128;
  constexpr int STAGE = KS * (A_SUB + B_SUB);
  constexpr int PA = KS * BM / 8, PB = KS * BN / 8, PT = PA + PB;
  constexpr int PHI = (PT + 7) / 8, NHI = PT - (PHI - 1) * 8;
  constexpr int LDA = KTOT;
  constexpr int LDB = 3 * D;
  LAS float* st_mean = (LAS float*)(lds + NS * STAGE);
  LAS float* st_rstd = st_mean + BM;
  LAS float* st_cs = st_rstd + BM;

  const int tid = threadIdx.x, lane = tid & 63;
  const int w = __builtin_amdgcn_readfirstlane(tid >> 6);
  const int wm = w % WM, wn = (w / WM) % WN, wk = w / (WM * WN);
  const int r16 = lane & 15, g = lane >> 4;
  const int row0 = rb * BM, col0 = cb * BN;
  auto mtile = [&](int m) { return STATS ? (m ^ wn) : m; };
  auto ntile = [&](int n) { int v = n + (STATS ? wm : 0); if (v >= NT) v -= NT; if (v >= NT) v -= NT; return v; };

  u32x2 resv[NT], biasv[NT];
  if (EPI == EPI_WO || EPI == EPI_FF2) {
#pragma unroll
    for (int nt = 0; nt < NT; nt++)
      resv[nt] = *(const u32x2*)(a.res + (size_t)(row0 + wm * 16 + r16) * D + col0 + nt * 16 + g * 4);
  }
  if (EPI == EPI_FF1 || EPI == EPI_FF2) {
#pragma unroll
    for (int nt = 0; nt < NT; nt++) biasv[nt] = *(const u32x2*)(a.bias + col0 + wn * NT * 16 + ntile(nt) * 16 + g * 4);
  }

  const char* src[PHI];
  int dst[PHI];
  int kstride[PHI];
#pragma unroll
  for (int i = 0; i < PHI; i++) {
    int p = w + i * 8;
    if (p >= PT) p = PT - 1;
    if (p < PA) {
      int sub = p / (BM / 8), pr = p % (BM / 8);
      int row = pr * 8 + (lane >> 3), ch = (lane & 7) ^ ((row >> 1) & 7);
      src[i] = (const char*)(a.A + (size_t)(row0 + row) * LDA + sub * 64 + ch * 8);
      dst[i] = sub * A_SUB + pr * 1024;
      kstride[i] = KS * 128;
    } else {
      int q = p - PA;
      if (RAWB) {
        int sub = q / (BN / 8), pq = q % (BN / 8);
        int nh = pq >> 3, kg = pq & 7;
        int row = lane >> 3, f = ((row >> 1) & 1) | ((kg & 1) << 1);
        int ch = (lane & 7) ^ (f << 1);
        src[i] = (const char*)(a.B + (size_t)(sub * 64 + kg * 8 + row) * LDB + col0 + nh * 64 + ch * 8);
        dst[i] = KS * A_SUB + sub * B_SUB + pq * 1024;
        kstride[i] = KS * 64 * LDB * 2;
      } else {
        src[i] = (const char*)a.B + (size_t)cb * (KTOT / 32) * (BN / 16) * 1024 + q * 1024 + lane * 16;
        dst[i] = KS * A_SUB + q * 1024;
        kstride[i] = PB * 1024;
      }
    }
  }
  auto issue = [&](int t) {
    int kt = t + rot;
    if (kt >= NKT) kt -= NKT;
    LAS char* sb = lds + (t % NS) * STAGE;
#pragma unroll
    for (int i = 0; i < PHI; i++)
      if (i < PHI - 1 || w < NHI) glds16(src[i] + (size_t)kt * kstride[i], sb + dst[i]);
  };

  f32x4 acc[MT][NT];
#pragma unroll
  for (int m = 0; m < MT; m++)
#pragma unroll
    for (int n = 0; n < NT; n++) acc[m][n] = (f32x4){0.f, 0.f, 0.f, 0.f};
  float s1 = 0.f, s2 = 0.f, cs = 0.f;

#pragma unroll
  for (int t = 0; t < NS - 1 && t < NKT; t++) issue(t);

  int aoff[MT];
#pragma unroll
  for (int m = 0; m < MT; m++) {
    int row = wm * MT * 16 + mtile(m) * 16 + r16;
    aoff[m] = row * 128 + ((g ^ ((row >> 1) & 7)) << 4);
  }
  int boff[NT];
#pragma unroll
  for (int n = 0; n < NT; n++) {
    if (RAWB) {
      int q = (lane >> 2) & 3, p = lane & 3, f = ((q >> 1) & 1) | ((g & 1) << 1);
      int nt = wn * NT + ntile(n);
      boff[n] = KS * A_SUB + (nt >> 2) * 8192 + (g * 8 + q) * 128 + ((((nt & 3) * 2 + (p >> 1)) ^ (f << 1)) << 4) + (p & 1) * 8;
    } else {
      boff[n] = KS * A_SUB + (wn * NT + ntile(n)) * 1024 + lane * 16;
    }
  }

#pragma unroll
  for (int t = 0; t < NKT; t++) {
    constexpr int dummy = 0; (void)dummy;
    {
      const int rem = (NKT - 1 - t) < (NS - 2) ? (NKT - 1 - t) : (NS - 2);
      if (NHI == 8 || w < NHI) {
        switch (rem) { case 0: wait_vm<0>(); break; case 1: wait_vm<PHI>(); break; case 2: wait_vm<2 * PHI>(); break;
                       case 3: wait_vm<3 * PHI>(); break; default: wait_vm<4 * PHI>(); break; }
      } else {
        switch (rem) { case 0: wait_vm<0>(); break; case 1: wait_vm<PHI - 1>(); break; case 2: wait_vm<2 * (PHI - 1)>(); break;
                       case 3: wait_vm<3 * (PHI - 1)>(); break; default: wait_vm<4 * (PHI - 1)>(); break; }
      }
    }
    bar();
    if (t + NS - 1 < NKT) issue(t + NS - 1);
    LAS char* sb = lds + (t % NS) * STAGE;
    u32x2 tr[2][NT][2];
    if (RAWB) {
#pragma unroll
      for (int n = 0; n < NT; n++) {
        unsigned ad = (unsigned)(size_t)(sb + boff[n]);
        asm volatile("ds_read_b64_tr_b16 %0, %4\n\tds_read_b64_tr_b16 %1, %4 offset:512\n\t"
                     "ds_read_b64_tr_b16 %2, %4 offset:4096\n\tds_read_b64_tr_b16 %3, %4 offset:4608"
                     : "=&v"(tr[0][n][0]), "=&v"(tr[0][n][1]), "=&v"(tr[1][n][0]), "=&v"(tr[1][n][1]) : "v"(ad));
      }
    }
    u32x4 afa[2 * KS / WK][MT];
#pragma unroll
    for (int kk = 0; kk < 2 * KS / WK; kk++) {
      const int ks = kk * WK + (WK > 1 ? wk : 0);
#pragma unroll
      for (int m = 0; m < MT; m++) afa[kk][m] = *(LAS u32x4*)(sb + (ks >> 1) * A_SUB + (aoff[m] ^ ((ks & 1) << 6)));
    }
    if (RAWB) {
      static_assert(!RAWB || NT == 4, "");
      asm volatile("s_waitcnt lgkmcnt(0)"
                   : "+v"(tr[0][0][0]), "+v"(tr[0][0][1]), "+v"(tr[1][0][0]), "+v"(tr[1][0][1]), "+v"(tr[0][1][0]), "+v"(tr[0][1][1]),
                     "+v"(tr[1][1][0]), "+v"(tr[1][1][1]), "+v"(tr[0][2 % NT][0]), "+v"(tr[0][2 % NT][1]), "+v"(tr[1][2 % NT][0]),
                     "+v"(tr[1][2 % NT][1]), "+v"(tr[0][3 % NT][0]), "+v"(tr[0][3 % NT][1]), "+v"(tr[1][3 % NT][0]), "+v"(tr[1][3 % NT][1]));
    }
#pragma unroll
    for (int kk = 0; kk < 2 * KS / WK; kk++) {
      const int ks = kk * WK + (WK > 1 ? wk : 0);
      u32x4 af[MT], bfr[NT];
#pragma unroll
      for (int m = 0; m < MT; m++) af[m] = afa[kk][m];
#pragma unroll
      for (int n = 0; n < NT; n++) {
        if (RAWB) bfr[n] = (u32x4){tr[kk & 1][n][0][0], tr[kk & 1][n][0][1], tr[kk & 1][n][1][0], tr[kk & 1][n][1][1]};
        else bfr[n] = *(LAS u32x4*)(sb + boff[n] + ks * (BN / 16) * 1024);
      }
#pragma unroll
      for (int m = 0; m < MT; m++)
#pragma unroll
        for (int n = 0; n < NT; n++) {
          bf16x8 x = __builtin_bit_cast(bf16x8, af[m]), y = __builtin_bit_cast(bf16x8, bfr[n]);
          acc[m][n] = SWAP ? __builtin_amdgcn_mfma_f32_16x16x32_bf16(y, x, acc[m][n], 0, 0, 0)
                           : __builtin_amdgcn_mfma_f32_16x16x32_bf16(x, y, acc[m][n], 0, 0, 0);
        }
      if (STATS) {
#pragma unroll
        for (int j = 0; j < 4; j++) {
          s1 = dot2(af[0][j], 0x3f803f80u, s1); s2 = dot2(af[0][j], af[0][j], s2); cs = dot2(bfr[0][j], 0x3f803f80u, cs);
        }
        asm volatile("" : "+v"(s1), "+v"(s2), "+v"(cs));
      }
    }
  }

  if (STATS) {
    s1 += __shfl_xor(s1, 16); s1 += __shfl_xor(s1, 32);
    s2 += __shfl_xor(s2, 16); s2 += __shfl_xor(s2, 32);
    cs += __shfl_xor(cs, 16); cs += __shfl_xor(cs, 32);
    float mean = s1 * (1.f / KTOT);
    float var = fmaxf(s2 * (1.f / KTOT) - mean * mean, 0.f);
    if (g == 0) {
      st_mean[wm * 32 + wn * 16 + r16] = mean;
      st_rstd[wm * 32 + wn * 16 + r16] = __builtin_amdgcn_rsqf(var + 1e-5f);
      if (wm < NT) st_cs[(wn * NT + wm) * 16 + r16] = cs;
    }
    bar();
  }

  if (EPI == EPI_QKV) {
    char* ws = a.out;
    if (SWAP) {
      const bool isq = cb < 5;
#pragma unroll
      for (int m = 0; m < MT; m++) {
        const int lr = wm * 32 + mtile(m) * 16 + r16, row = row0 + lr;
        const float mean = st_mean[lr];
        const float rs = st_rstd[lr] * (isq ? 0.22360679774997896f * 1.4426950408889634f : 1.f);
#pragma unroll
        for (int n = 0; n < NT; n++) {
          const int lc = (wn * NT + ntile(n)) * 16 + g * 4;
          const f32x4 c4 = *(LAS f32x4*)(st_cs + lc);
          u32x2 o;
          o[0] = pk2(rs * (acc[m][n][0] - mean * c4[0]), rs * (acc[m][n][1] - mean * c4[1]));
          o[1] = pk2(rs * (acc[m][n][2] - mean * c4[2]), rs * (acc[m][n][3] - mean * c4[3]));
          const unsigned cc = col0 + lc - (isq ? 0 : D);
          const unsigned head = cc / 20u, d0 = cc - head * 20u;
          if (isq) {
            *(u32x2*)(ws + WS_Q + (size_t)(head * S + row) * 48 + d0 * 2) = o;
          } else {
            char* t = ws + WS_KV + (size_t)head * KV_HEAD + (row >> 5) * KV_TILE;
            const int key = row & 31;
            if (d0 < 16) {
              *(u32x2*)(t + (d0 >> 3) * 512 + key * 16 + (d0 & 4) * 2) = o;
            } else {
              *(u32x2*)(t + 1024 + key * 8) = o;
              *(u32x2*)(t + 1280 + key * 8) = (u32x2){0x3f803f80u, 0x00003f80u};
            }
          }
        }
      }
    } else {
#pragma unroll
      for (int m = 0; m < MT; m++) {
        const int lr = wm * 32 + mtile(m) * 16 + g * 4;
        const f32x4 mean = *(LAS f32x4*)(st_mean + lr), rs = *(LAS f32x4*)(st_rstd + lr);
        const int row16 = (row0 + wm * 32 + mtile(m) * 16) >> 4;
#pragma unroll
        for (int n = 0; n < NT; n++) {
          const int lc = (wn * NT + ntile(n)) * 16 + r16;
          const float c = st_cs[lc];
          u32x2 o;
          o[0] = pk2(rs[0] * (acc[m][n][0] - mean[0] * c), rs[1] * (acc[m][n][1] - mean[1] * c));
          o[1] = pk2(rs[2] * (acc[m][n][2] - mean[2] * c), rs[3] * (acc[m][n][3] - mean[3] * c));
          const unsigned cc = col0 + lc - 2 * D;
          const unsigned head = cc / 20u, d = cc - head * 20u;
          char* t = ws + WS_KV + (size_t)head * KV_HEAD + (row16 >> 1) * KV_TILE + 1536 + (row16 & 1) * 672 + (g & 1) * 336 + (g >> 1) * 8;
          *(u32x2*)(t + d * 16) = o;
          if (d == 0) *(u32x2*)(t + 320) = (u32x2){0x3f803f80u, 0x3f803f80u};
        }
      }
    }
  } else if (EPI == EPI_FF1) {
#pragma unroll
    for (int m = 0; m < MT; m++) {
      const int lr = wm * 32 + mtile(m) * 16 + r16, row = row0 + lr;
      const float mean = st_mean[lr], rs = st_rstd[lr];
#pragma unroll
      for (int n = 0; n < NT; n++) {
        const int lc = (wn * NT + ntile(n)) * 16 + g * 4;
        const f32x4 c4 = *(LAS f32x4*)(st_cs + lc);
        float v0 = fmaxf(rs * (acc[m][n][0] - mean * c4[0]) + bflo(biasv[n][0]), 0.f);
        float v1 = fmaxf(rs * (acc[m][n][1] - mean * c4[1]) + bfhi(biasv[n][0]), 0.f);
        float v2 = fmaxf(rs * (acc[m][n][2] - mean * c4[2]) + bflo(biasv[n][1]), 0.f);
        float v3 = fmaxf(rs * (acc[m][n][3] - mean * c4[3]) + bfhi(biasv[n][1]), 0.f);
        u32x2 o = {pk2(v0 * v0, v1 * v1), pk2(v2 * v2, v3 * v3)};
        *(u32x2*)(a.out + ((size_t)row * FF + col0 + lc) * 2) = o;
      }
    }
  } else {
    bar();
    LAS f32x4* red = (LAS f32x4*)lds + wm * NT * 64 + lane;
    if (wk == 1) {
#pragma unroll
      for (int n = 0; n < NT; n++) red[n * 64] = acc[0][n];
    }
    bar();
    if (wk == 0) {
      const int row = row0 + wm * 16 + r16;
#pragma unroll
      for (int n = 0; n < NT; n++) {
        f32x4 v = acc[0][n] + red[n * 64];
        if (EPI == EPI_FF2) {
          v[0] += bflo(biasv[n][0]); v[1] += bfhi(biasv[n][0]); v[2] += bflo(biasv[n][1]); v[3] += bfhi(biasv[n][1]);
        }
        u32x2 o = {pk2(v[0] + bflo(resv[n][0]), v[1] + bfhi(resv[n][0])), pk2(v[2] + bflo(resv[n][1]), v[3] + bfhi(resv[n][1]))};
        *(u32x2*)(a.out + ((size_t)row * D + col0 + n * 16 + g * 4) * 2) = o;
      }
    }
  }
}

constexpr int LDS_QKV = 4 * 32768 + 2048;
constexpr int LDS_FF1 = 5 * 28672 + 2048;
constexpr int LDS_N80 = 4 * 36864;

__global__ __launch_bounds__(512, 2) void k_qkv(GemmArgs a) {
  extern __shared__ __attribute__((aligned(16))) char smem[];
  const int xcd = blockIdx.x & 7, i = blockIdx.x >> 3;
  const int rb = (xcd & 3) * 4 + (i & 3), cb = (xcd >> 2) * 8 + (i >> 2);
  if (cb >= 15) return;
  const int rot = (rb + cb) % 10;
  if (cb < 10) gemm_body<128, 128, 640, 1, 4, 2, 1, 4, true, true, EPI_QKV, true>(a, rb, cb, rot, (LAS char*)smem);
  else gemm_body<128, 128, 640, 1, 4, 2, 1, 4, true, true, EPI_QKV, false>(a, rb, cb, rot, (LAS char*)smem);
}
__global__ __launch_bounds__(512, 2) void k_wo(GemmArgs a) {
  extern __shared__ __attribute__((aligned(16))) char smem[];
  const int xcd = blockIdx.x & 7, i = blockIdx.x >> 3;
  gemm_body<64, 80, 640, 2, 4, 1, 2, 4, false, false, EPI_WO, true>(a, xcd * 4 + (i & 3), i >> 2, 0, (LAS char*)smem);
}
__global__ __launch_bounds__(512, 2) void k_ff1(GemmArgs a) {
  extern __shared__ __attribute__((aligned(16))) char smem[];
  const int xcd = blockIdx.x & 7, i = blockIdx.x >> 3;
  gemm_body<128, 96, 640, 1, 4, 2, 1, 5, false, true, EPI_FF1, true>(a, xcd * 2 + (i & 1), i >> 1, 0, (LAS char*)smem);
}
__global__ __launch_bounds__(512, 2) void k_ff2(GemmArgs a) {
  extern __shared__ __attribute__((aligned(16))) char smem[];
  const int xcd = blockIdx.x & 7, i = blockIdx.x >> 3;
  gemm_body<64, 80, 1536, 2, 4, 1, 2, 4, false, false, EPI_FF2, true>(a, xcd * 4 + (i & 3), i >> 2, 0, (LAS char*)smem);
}

struct AttnArgs {
  char* ws;
  const bf16* wo;
  const bf16* w1;
  const bf16* w2;
};
constexpr int AT_STAGE = 16 * KV_TILE;
constexpr int LDS_ATT = 3 * AT_STAGE;

__device__ __forceinline__ f32x16 qk_tile(u32x4 klo, u32x2 khi, u32x4 qlo, u32x2 qhi) {
  f32x16 z = {0.f, 0.f, 0.f, 0.f, 0.f, 0.f, 0.f, 0.f, 0.f, 0.f, 0.f, 0.f, 0.f, 0.f, 0.f, 0.f};
  f32x16 s = __builtin_amdgcn_mfma_f32_32x32x16_bf16(__builtin_bit_cast(bf16x8, klo), __builtin_bit_cast(bf16x8, qlo), z, 0, 0, 0);
  return __builtin_amdgcn_mfma_f32_32x32x8bf16_1k(__builtin_bit_cast(s16x4, khi), __builtin_bit_cast(s16x4, qhi), s, 0, 0, 0);
}
__device__ __forceinline__ f32x16 pv_tile(const f32x16& s, u32x4 v0, u32x4 v1, f32x16 o) {
  u32x4 p0, p1;
#pragma unroll
  for (int i = 0; i < 4; i++) {
    p0[i] = pk2(__builtin_amdgcn_exp2f(s[2 * i]), __builtin_amdgcn_exp2f(s[2 * i + 1]));
    p1[i] = pk2(__builtin_amdgcn_exp2f(s[8 + 2 * i]), __builtin_amdgcn_exp2f(s[9 + 2 * i]));
  }
  o = __builtin_amdgcn_mfma_f32_32x32x16_bf16(__builtin_bit_cast(bf16x8, v0), __builtin_bit_cast(bf16x8, p0), o, 0, 0, 0);
  return __builtin_amdgcn_mfma_f32_32x32x16_bf16(__builtin_bit_cast(bf16x8, v1), __builtin_bit_cast(bf16x8, p1), o, 0, 0, 0);
}
__device__ __forceinline__ float max16(const f32x16& s) {
  float m = fmaxf(fmaxf(s[0], s[1]), s[2]);
#pragma unroll
  for (int i = 3; i < 15; i += 2) m = fmaxf(fmaxf(m, s[i]), s[i + 1]);
  return fmaxf(m, s[15]);
}
__device__ __forceinline__ u32x2 set_shift(u32x2 qhi, float m, int hh) {
  float a = bflo(pk2(m, 0.f) & 0xffffu);
  float r = m - a;
  float b = bflo(pk2(r, 0.f) & 0xffffu);
  float c = r - b;
  u32x2 e = {pk2(-a, -b), pk2(-c, 0.f)};
  return hh ? e : qhi;
}

__global__ __launch_bounds__(1024, 4) void k_attn(AttnArgs a) {
  extern __shared__ __attribute__((aligned(16))) char smem[];
  LAS char* lds = (LAS char*)smem;
  const int tid = threadIdx.x, lane = tid & 63;
  const int w = __builtin_amdgcn_readfirstlane(tid >> 6);
  const int xcd = blockIdx.x & 7, bi = blockIdx.x >> 3;
  const int head = xcd * 4 + (bi & 3), qb = bi >> 2;
  const int r = lane & 31, hh = lane >> 5, qt = w & 7, half = w >> 3;
  const int qrow = qb * 256 + qt * 32 + r;
  const char* kvg = a.ws + WS_KV + (size_t)head * KV_HEAD;

  const char* qp = a.ws + WS_Q + (size_t)(head * S + qrow) * 48;
  u32x4 qlo = *(const u32x4*)(qp + hh * 16);
  u32x2 qhi0 = *(const u32x2*)(qp + 32);

  auto issue = [&](int s) {
    const char* gsrc = kvg + (size_t)s * AT_STAGE + lane * 16;
    LAS char* sb = lds + (s % 3) * AT_STAGE;
    glds16(gsrc + w * 1024, sb + w * 1024);
    glds16(gsrc + (w + 16) * 1024, sb + (w + 16) * 1024);
    if (w < 13) glds16(gsrc + (w + 32) * 1024, sb + (w + 32) * 1024);
  };
  issue(0); issue(1); issue(2);

  {
    const int n16 = lane & 15, g = lane >> 4;
    for (int f = blockIdx.x * 16 + w; f < 4640; f += 4096) {
      const bf16* W; char* dstp; int ld, ks, nt, per, nks;
      if (f < 800) { W = a.wo; dstp = a.ws + WS_WO; ld = D; ks = f / 40; nt = f % 40; per = 5; nks = 20; }
      else if (f < 2720) { int e = f - 800; W = a.w1; dstp = a.ws + WS_W1; ld = FF; ks = e / 96; nt = e % 96; per = 6; nks = 20; }
      else { int e = f - 2720; W = a.w2; dstp = a.ws + WS_W2; ld = D; ks = e / 40; nt = e % 40; per = 5; nks = 48; }
      const unsigned short* p = (const unsigned short*)W + (size_t)(ks * 32 + g * 8) * ld + nt * 16 + n16;
      unsigned v[8];
#pragma unroll
      for (int j = 0; j < 8; j++) v[j] = p[(size_t)j * ld];
      u32x4 o = {v[0] | (v[1] << 16), v[2] | (v[3] << 16), v[4] | (v[5] << 16), v[6] | (v[7] << 16)};
      const int cbk = nt / per, nin = nt % per;
      *(u32x4*)(dstp + ((size_t)(cbk * nks + ks) * per + nin) * 1024 + lane * 16) = o;
    }
  }
  wait_vm<0>();
  bar();

  const int rv = r <= 20 ? r : (r < 28 ? 20 : 19);
  const int koff = lane * 16, khoff = 1024 + lane * 8, voff = 1536 + hh * 336 + rv * 16;
  const u32x2 qz = hh ? (u32x2){0u, 0u} : qhi0;
  u32x2 qhi;
  {
    f32x16 s = qk_tile(*(LAS u32x4*)(lds + koff), *(LAS u32x2*)(lds + khoff), qlo, qz);
    float m = max16(s);
    m = fmaxf(m, __shfl_xor(m, 32));
    qhi = set_shift(qhi0, m, hh);
  }
  f32x16 o = {0.f, 0.f, 0.f, 0.f, 0.f, 0.f, 0.f, 0.f, 0.f, 0.f, 0.f, 0.f, 0.f, 0.f, 0.f, 0.f};
#pragma unroll 1
  for (int s = 0; s < 4; s++) {
    if (s == 1) { bar(); issue(3); }
    if (s == 3) { wait_vm<0>(); bar(); }
    LAS char* tb = lds + (s == 3 ? 0 : s) * AT_STAGE + half * 8 * KV_TILE;
#pragma unroll
    for (int t = 0; t < 8; t++) {
      LAS char* p = tb + t * KV_TILE;
      f32x16 sc = qk_tile(*(LAS u32x4*)(p + koff), *(LAS u32x2*)(p + khoff), qlo, qhi);
      o = pv_tile(sc, *(LAS u32x4*)(p + voff), *(LAS u32x4*)(p + voff + 672), o);
    }
  }
  LAS f32x4* red = (LAS f32x4*)(lds + AT_STAGE) + qt * 192 + lane;
  if (half == 1) {
    red[0] = (f32x4){o[0], o[1], o[2], o[3]};
    red[64] = (f32x4){o[4], o[5], o[6], o[7]};
    red[128] = (f32x4){o[8], o[9], o[10], o[11]};
  }
  bar();
  if (half == 0) {
    f32x4 x0 = red[0], x1 = red[64], x2 = red[128];
#pragma unroll
    for (int i = 0; i < 4; i++) { o[i] += x0[i]; o[4 + i] += x1[i]; o[8 + i] += x2[i]; }
    float l = __shfl(o[8], r + 32);
    if (!__all(l < 1.8446744e19f)) {
      float m = -__builtin_inff();
#pragma unroll 1
      for (int t = 0; t < 64; t++) {
        const char* p = kvg + t * KV_TILE;
        f32x16 sc = qk_tile(*(const u32x4*)(p + koff), *(const u32x2*)(p + khoff), qlo, qz);
        m = fmaxf(m, max16(sc));
      }
      m = fmaxf(m, __shfl_xor(m, 32));
      qhi = set_shift(qhi0, m, hh);
#pragma unroll
      for (int i = 0; i < 16; i++) o[i] = 0.f;
#pragma unroll 1
      for (int t = 0; t < 64; t++) {
        const char* p = kvg + t * KV_TILE;
        f32x16 sc = qk_tile(*(const u32x4*)(p + koff), *(const u32x2*)(p + khoff), qlo, qhi);
        o = pv_tile(sc, *(const u32x4*)(p + voff), *(const u32x4*)(p + voff + 672), o);
      }
      l = __shfl(o[8], r + 32);
    }
    const float inv = 1.f / l;
    char* op = a.ws + WS_O + ((size_t)qrow * D + head * DH + hh * 4) * 2;
    *(u32x2*)(op) = (u32x2){pk2(o[0] * inv, o[1] * inv), pk2(o[2] * inv, o[3] * inv)};
    *(u32x2*)(op + 16) = (u32x2){pk2(o[4] * inv, o[5] * inv), pk2(o[6] * inv, o[7] * inv)};
    if (hh == 0) *(u32x2*)(op + 32) = (u32x2){pk2(o[8] * inv, o[9] * inv), pk2(o[10] * inv, o[11] * inv)};
  }
}

extern "C" void kernel_launch(void* const* d_in, const int* in_sizes, int n_in, void* d_out, int out_size, void* d_ws,
                              size_t ws_size, hipStream_t stream) {
  const bf16* x = (const bf16*)d_in[0];
  const bf16* wqkv = (const bf16*)d_in[1];
  const bf16* wo = (const bf16*)d_in[2];
  const bf16* w1 = (const bf16*)d_in[4];
  const bf16* b1 = (const bf16*)d_in[5];
  const bf16* w2 = (const bf16*)d_in[6];
  const bf16* b2 = (const bf16*)d_in[7];
  char* ws = (char*)d_ws;
  (void)hipFuncSetAttribute((const void*)k_qkv, hipFuncAttributeMaxDynamicSharedMemorySize, LDS_QKV);
  (void)hipFuncSetAttribute((const void*)k_attn, hipFuncAttributeMaxDynamicSharedMemorySize, LDS_ATT);
  (void)hipFuncSetAttribute((const void*)k_wo, hipFuncAttributeMaxDynamicSharedMemorySize, LDS_N80);
  (void)hipFuncSetAttribute((const void*)k_ff1, hipFuncAttributeMaxDynamicSharedMemorySize, LDS_FF1);
  (void)hipFuncSetAttribute((const void*)k_ff2, hipFuncAttributeMaxDynamicSharedMemorySize, LDS_N80);

  GemmArgs g1 = {x, wqkv, x, b1, ws};
  k_qkv<<<256, 512, LDS_QKV, stream>>>(g1);
  AttnArgs at = {ws, wo, w1, w2};
  k_attn<<<256, 1024, LDS_ATT, stream>>>(at);
  GemmArgs g3 = {(const bf16*)(ws + WS_O), (const bf16*)(ws + WS_WO), x, b1, ws + WS_H};
  k_wo<<<256, 512, LDS_N80, stream>>>(g3);
  GemmArgs g4 = {(const bf16*)(ws + WS_H), (const bf16*)(ws + WS_W1), x, b1, ws + WS_G};
  k_ff1<<<256, 512, LDS_FF1, stream>>>(g4);
  GemmArgs g5 = {(const bf16*)(ws + WS_G), (const bf16*)(ws + WS_W2), (const bf16*)(ws + WS_H), b2, (char*)d_out};
  k_ff2<<<256, 512, LDS_N80, stream>>>(g5);
}
```

```cpp
#include <hip/hip_runtime.h>
#include <hip/hip_bf16.h>
#include <stdint.h>
#include <type_traits>

#define LAS __attribute__((address_space(3)))
#define GAS __attribute__((address_space(1)))
typedef __bf16 bf16;
typedef unsigned long long u64;
typedef __bf16 bf16x2 __attribute__((ext_vector_type(2)));
typedef __bf16 bf16x8 __attribute__((ext_vector_type(8)));
typedef short s16x4 __attribute__((ext_vector_type(4)));
typedef float f32x2 __attribute__((ext_vector_type(2)));
typedef float f32x4 __attribute__((ext_vector_type(4)));
typedef float f32x16 __attribute__((ext_vector_type(16)));
typedef unsigned u32x2 __attribute__((ext_vector_type(2)));
typedef unsigned u32x4 __attribute__((ext_vector_type(4)));
typedef __amdgpu_buffer_rsrc_t rsrc_t;

constexpr int S = 2048, D = 640, NH = 32, DH = 20, FF = 1536;
constexpr int KV_TILE = 2944;
constexpr int KV_HEAD = 64 * KV_TILE;
constexpr int AT_STAGE = 16 * KV_TILE;
constexpr int LDS_BYTES = 163840;
constexpr unsigned WS_FLAG = 0;
constexpr unsigned F_QKV = 0, F_ATT = 256, F_WO = 512, F_FF1 = 768, F_RT = 1024, F_EPOCH = 1536;
constexpr unsigned WS_Q = 16384;
constexpr unsigned WS_KV = WS_Q + NH * S * 40;
constexpr unsigned WS_O = WS_KV + NH * KV_HEAD;
constexpr unsigned WS_H = WS_O + S * D * 2;
constexpr unsigned WS_G = WS_H + S * D * 2;
constexpr unsigned WS_WO = WS_G + S * FF * 2;
constexpr unsigned WS_W1 = WS_WO + D * D * 2;
constexpr unsigned WS_W2 = WS_W1 + D * FF * 2;
constexpr unsigned WS_SP = WS_W2 + D * FF * 2;
constexpr unsigned WS_END = WS_SP + S * 64;

#define SB() __builtin_amdgcn_sched_barrier(0)
template <int N> __device__ __forceinline__ void wait_vm() { asm volatile("s_waitcnt vmcnt(%0)" ::"n"(N) : "memory"); }
__device__ __forceinline__ void bar() { asm volatile("s_waitcnt lgkmcnt(0)" ::: "memory"); __builtin_amdgcn_s_barrier(); asm volatile("" ::: "memory"); }
__device__ __forceinline__ void rawbar() { asm volatile("" ::: "memory"); __builtin_amdgcn_s_barrier(); asm volatile("" ::: "memory"); }
template <int AUX> __device__ __forceinline__ void glds16(const void* g, LAS char* l) {
  __builtin_amdgcn_global_load_lds((const GAS void*)g, (LAS void*)l, 16, 0, AUX);
}
__device__ __forceinline__ void st16(rsrc_t r, unsigned off, u32x4 v) { __builtin_amdgcn_raw_buffer_store_b128(v, r, off, 0, 16); }
__device__ __forceinline__ void st8(rsrc_t r, unsigned off, u32x2 v) { __builtin_amdgcn_raw_buffer_store_b64(v, r, off, 0, 16); }
__device__ __forceinline__ unsigned pk2(float a, float b) {
  f32x2 f = {a, b};
  return __builtin_bit_cast(unsigned, __builtin_convertvector(f, bf16x2));
}
__device__ __forceinline__ float bflo(unsigned u) { return __builtin_bit_cast(float, u << 16); }
__device__ __forceinline__ float bfhi(unsigned u) { return __builtin_bit_cast(float, u & 0xffff0000u); }
__device__ __forceinline__ f32x4 mma16(u32x4 a, u32x4 b, f32x4 c) {
  return __builtin_amdgcn_mfma_f32_16x16x32_bf16(__builtin_bit_cast(bf16x8, a), __builtin_bit_cast(bf16x8, b), c, 0, 0, 0);
}
template <int I, int N, class F> __device__ __forceinline__ void static_for(F&& f) {
  if constexpr (I < N) { f(std::integral_constant<int, I>{}); static_for<I + 1, N>(f); }
}

struct Params {
  const bf16 *x, *wqkv, *wo, *w1, *b1, *w2, *b2;
  bf16* out;
  char* ws;
};
struct Ctx {
  int tid, lane, w, r16, g;
  LAS char* lds;
  char* ws;
  rsrc_t rs;
  u64 token;
};
#define STAMP(i) do {} while (0)

__device__ __forceinline__ void publish(Ctx& c, unsigned flag) {
  wait_vm<0>();
  bar();
  if (c.tid == 0) __hip_atomic_store((u64*)(c.ws + WS_FLAG) + flag, c.token, __ATOMIC_RELAXED, __HIP_MEMORY_SCOPE_AGENT);
}
__device__ __forceinline__ void spin_flags(Ctx& c, unsigned idx0, int n0, unsigned idx1, int n1) {
  const u64* f = (const u64*)(c.ws + WS_FLAG);
  for (int it = 0; it < (1 << 20); it++) {
    u64 a = c.token, b = c.token;
    if (c.lane < n0) a = __hip_atomic_load(f + idx0, __ATOMIC_RELAXED, __HIP_MEMORY_SCOPE_AGENT);
    if (c.lane < n1) b = __hip_atomic_load(f + idx1, __ATOMIC_RELAXED, __HIP_MEMORY_SCOPE_AGENT);
    if (__all(a == c.token && b == c.token)) break;
    __builtin_amdgcn_s_sleep(1);
  }
}
__device__ __forceinline__ void await(Ctx& c, unsigned idx0, int n0, unsigned idx1, int n1) {
  if (c.w == 7) spin_flags(c, idx0, n0, idx1, n1);
  rawbar();
}

template <bool ISV>
__device__ __forceinline__ void qkv_phase(Ctx& c, const Params& p, int rb, int cb) {
  constexpr int NS = 4, NKT = 10, STAGE = 32768, LDB = 3 * D;
  LAS char* lds = c.lds;
  LAS float* st_mean = (LAS float*)(lds + NS * STAGE);
  LAS float* st_rstd = st_mean + 128;
  LAS float* st_cs = st_rstd + 128;
  const int lane = c.lane, w = c.w, r16 = c.r16, g = c.g;
  const int wm = w & 3, wn = w >> 2;
  const int row0 = rb * 128, col0 = cb * 128;
  const int rot = (rb + cb) % NKT;

  const char* srcA[2];
  const char* srcB[2];
#pragma unroll
  for (int i = 0; i < 2; i++) {
    const int pc = w + 8 * i;
    {
      const int lr = pc * 8 + (lane >> 3), rho = lr & 31;
      const int gr = ISV ? (16 * ((rho >> 3) & 1) + 8 * (rho >> 4) + (rho & 7)) : (2 * (rho & 15) + (rho >> 4));
      const int ch = (lane & 7) ^ ((lr >> 1) & 7);
      srcA[i] = (const char*)(p.x + (size_t)(row0 + (lr & ~31) + gr) * D + ch * 8);
    }
    {
      const int row = lane >> 3, f = ((row >> 1) & 1) | ((w & 1) << 1);
      const int ch = (lane & 7) ^ (f << 1);
      srcB[i] = (const char*)(p.wqkv + (size_t)(w * 8 + row) * LDB + col0 + i * 64 + ch * 8);
    }
  }
  auto issue = [&](int t, int i) {
    int kt = t + rot;
    if (kt >= NKT) kt -= NKT;
    LAS char* sb = lds + (t % NS) * STAGE;
    if (i < 2) glds16<0>(srcA[i] + kt * 128, sb + (w + 8 * i) * 1024);
    else glds16<0>(srcB[i - 2] + (size_t)kt * (64 * LDB * 2), sb + 16384 + (w + 8 * (i - 2)) * 1024);
  };
#pragma unroll
  for (int t = 0; t < NS; t++)
#pragma unroll
    for (int i = 0; i < 4; i++) issue(t, i);

  int aoff[2], boff[4];
#pragma unroll
  for (int m = 0; m < 2; m++) {
    const int row = wm * 32 + (m ^ wn) * 16 + r16;
    aoff[m] = row * 128 + ((g ^ ((row >> 1) & 7)) << 4);
  }
#pragma unroll
  for (int n = 0; n < 4; n++) {
    const int q = (lane >> 2) & 3, pp = lane & 3, f = ((q >> 1) & 1) | ((g & 1) << 1);
    const int nt = (n + wm) & 3;
    boff[n] = 16384 + wn * 8192 + (g * 8 + q) * 128 + (((nt * 2 + (pp >> 1)) ^ (f << 1)) << 4) + (pp & 1) * 8;
  }
  f32x4 acc[2][4], a_s1 = {0.f, 0.f, 0.f, 0.f}, a_s2 = a_s1, a_cs = a_s1;
#pragma unroll
  for (int m = 0; m < 2; m++)
#pragma unroll
    for (int n = 0; n < 4; n++) acc[m][n] = a_s1;
  const u32x4 ones = {0x3f803f80u, 0x3f803f80u, 0x3f803f80u, 0x3f803f80u};
  u32x4 af[2][2][2];
  u32x2 bq[2][2][4][2];
  auto read1 = [&](int t, int r) {
    LAS char* sb = lds + (t % NS) * STAGE;
    const int bi = t & 1;
    if (r < 8) {
      const int kk = r >> 2, n = r & 3;
      const unsigned ad = (unsigned)(size_t)(sb + boff[n]);
      if (kk == 0) asm volatile("ds_read_b64_tr_b16 %0, %2\n\tds_read_b64_tr_b16 %1, %2 offset:512"
                                : "=&v"(bq[bi][0][n][0]), "=&v"(bq[bi][0][n][1]) : "v"(ad));
      else asm volatile("ds_read_b64_tr_b16 %0, %2 offset:4096\n\tds_read_b64_tr_b16 %1, %2 offset:4608"
                        : "=&v"(bq[bi][1][n][0]), "=&v"(bq[bi][1][n][1]) : "v"(ad));
    } else {
      const int kk = (r - 8) >> 1, m = r & 1;
      af[bi][kk][m] = *(LAS u32x4*)(sb + (aoff[m] ^ (kk << 6)));
    }
  };
  wait_vm<12>();
  bar();
  STAMP(1);
#pragma unroll
  for (int r = 0; r < 12; r++) read1(0, r);
  static_for<0, NKT>([&](auto tc) {
    constexpr int t = tc.value, bi = t & 1;
    if constexpr (t + 1 < NKT) wait_vm<4 * ((NKT - 2 - t) < (NS - 2) ? (NKT - 2 - t) : (NS - 2))>();
    asm volatile("s_waitcnt lgkmcnt(0)"
                 : "+v"(bq[bi][0][0][0]), "+v"(bq[bi][0][0][1]), "+v"(bq[bi][1][0][0]), "+v"(bq[bi][1][0][1]),
                   "+v"(bq[bi][0][1][0]), "+v"(bq[bi][0][1][1]), "+v"(bq[bi][1][1][0]), "+v"(bq[bi][1][1][1]),
                   "+v"(bq[bi][0][2][0]), "+v"(bq[bi][0][2][1]), "+v"(bq[bi][1][2][0]), "+v"(bq[bi][1][2][1]),
                   "+v"(bq[bi][0][3][0]), "+v"(bq[bi][0][3][1]), "+v"(bq[bi][1][3][0]), "+v"(bq[bi][1][3][1]));
    if constexpr (t + 1 < NKT) bar();
    SB();
#pragma unroll
    for (int i = 0; i < 22; i++) {
      const int kk = i / 11, j = i % 11;
      if (j < 8) {
        const int m = j >> 2, n = j & 3;
        const u32x4 bv = {bq[bi][kk][n][0][0], bq[bi][kk][n][0][1], bq[bi][kk][n][1][0], bq[bi][kk][n][1][1]};
        acc[m][n] = ISV ? mma16(af[bi][kk][m], bv, acc[m][n]) : mma16(bv, af[bi][kk][m], acc[m][n]);
      } else if (j == 8) {
        a_s1 = mma16(ones, af[bi][kk][0], a_s1);
      } else if (j == 9) {
        a_s2 = mma16(af[bi][kk][0], af[bi][kk][0], a_s2);
      } else {
        const u32x4 bv = {bq[bi][kk][0][0][0], bq[bi][kk][0][0][1], bq[bi][kk][0][1][0], bq[bi][kk][0][1][1]};
        a_cs = mma16(ones, bv, a_cs);
      }
      if (t + 1 < NKT && i < 18 && (i % 3) != 2) {
        const int r = (i / 3) * 2 + (i % 3);
        read1(t + 1, r < 4 ? r : (r < 6 ? r + 4 : (r < 10 ? r - 2 : r)));
      }
      if (t + NS < NKT && (i % 5) == 2 && i / 5 < 4) issue(t + NS, i / 5);
      SB();
    }
  });
  STAMP(2);
  {
    const float mean = a_s1[0] * (1.f / D);
    const int e = r16 & 3;
    const float s2 = e == 0 ? a_s2[0] : (e == 1 ? a_s2[1] : (e == 2 ? a_s2[2] : a_s2[3]));
    const float var = fmaxf(s2 * (1.f / D) - mean * mean, 0.f);
    if (g == (r16 >> 2)) {
      st_mean[wm * 32 + wn * 16 + r16] = mean;
      st_rstd[wm * 32 + wn * 16 + r16] = __builtin_amdgcn_rsqf(var + 1e-5f);
      st_cs[(wn * 4 + wm) * 16 + r16] = a_cs[0];
    }
    bar();
  }
  const int tile = (row0 >> 5) + wm;
  if (!ISV) {
    const bool isq = cb < 5;
    float mean[2], rs[2];
#pragma unroll
    for (int m = 0; m < 2; m++) {
      mean[m] = st_mean[wm * 32 + (m ^ wn) * 16 + r16];
      rs[m] = st_rstd[wm * 32 + (m ^ wn) * 16 + r16] * (isq ? 0.22360679774997896f * 1.4426950408889634f : 1.f);
    }
#pragma unroll
    for (int n = 0; n < 4; n++) {
      const int lc = (wn * 4 + ((n + wm) & 3)) * 16 + g * 4;
      const f32x4 c4 = *(LAS f32x4*)(st_cs + lc);
      u32x2 o[2];
#pragma unroll
      for (int m = 0; m < 2; m++) {
        o[m][0] = pk2(rs[m] * (acc[m][n][0] - mean[m] * c4[0]), rs[m] * (acc[m][n][1] - mean[m] * c4[1]));
        o[m][1] = pk2(rs[m] * (acc[m][n][2] - mean[m] * c4[2]), rs[m] * (acc[m][n][3] - mean[m] * c4[3]));
      }
      const u32x4 v = wn ? (u32x4){o[1][0], o[1][1], o[0][0], o[0][1]} : (u32x4){o[0][0], o[0][1], o[1][0], o[1][1]};
      const unsigned cc = col0 + lc - (isq ? 0 : D);
      if (isq) {
        st16(c.rs, WS_Q + ((cc >> 2) * S + tile * 32 + 2 * r16) * 8, v);
      } else {
        const unsigned head = cc / 20u, d0 = cc - head * 20u;
        const unsigned t = WS_KV + head * KV_HEAD + tile * KV_TILE + r16 * 16;
        st16(c.rs, t + d0 * 64, v);
        if (d0 == 16) st16(c.rs, t + 1280, (u32x4){0x3f803f80u, 0x00003f80u, 0x3f803f80u, 0x00003f80u});
      }
    }
  } else {
    f32x4 mean[2], rs[2];
#pragma unroll
    for (int m = 0; m < 2; m++) {
      mean[m] = *(LAS f32x4*)(st_mean + wm * 32 + (m ^ wn) * 16 + g * 4);
      rs[m] = *(LAS f32x4*)(st_rstd + wm * 32 + (m ^ wn) * 16 + g * 4);
    }
#pragma unroll
    for (int n = 0; n < 4; n++) {
      const int lc = (wn * 4 + ((n + wm) & 3)) * 16 + r16;
      const float cs = st_cs[lc];
      u32x2 o[2];
#pragma unroll
      for (int m = 0; m < 2; m++) {
        o[m][0] = pk2(rs[m][0] * (acc[m][n][0] - mean[m][0] * cs), rs[m][1] * (acc[m][n][1] - mean[m][1] * cs));
        o[m][1] = pk2(rs[m][2] * (acc[m][n][2] - mean[m][2] * cs), rs[m][3] * (acc[m][n][3] - mean[m][3] * cs));
      }
      const u32x4 v = wn ? (u32x4){o[1][0], o[1][1], o[0][0], o[0][1]} : (u32x4){o[0][0], o[0][1], o[1][0], o[1][1]};
      const unsigned cc = col0 + lc - 2 * D;
      const unsigned head = cc / 20u, d = cc - head * 20u;
      const unsigned t = WS_KV + head * KV_HEAD + tile * KV_TILE + 1536 + g * 352;
      st16(c.rs, t + d * 16, v);
      if (d < 2) { const unsigned e = d ? 0u : 0x3f803f80u; st16(c.rs, t + 320 + d * 16, (u32x4){e, e, e, e}); }
    }
  }
}

__device__ __forceinline__ void retile(Ctx& c, const Params& p, int job) {
  const bf16* W; unsigned dst; int ld, ks, grp, nks; bool n80 = true;
  if (job < 200) { W = p.wo; dst = WS_WO; ld = D; ks = job / 10; grp = job % 10; nks = 20; }
  else if (job < 680) { const int e = job - 200; W = p.w1; dst = WS_W1; ld = FF; ks = e / 24; grp = e % 24; nks = 20; n80 = false; }
  else { const int e = job - 680; W = p.w2; dst = WS_W2; ld = D; ks = e / 10; grp = e % 10; nks = 48; }
  const int col = grp * 64 + c.r16 * 4;
  const bf16* src = W + (size_t)(ks * 32 + c.g * 8) * ld + col;
  u32x2 v[8];
#pragma unroll
  for (int j = 0; j < 8; j++) v[j] = *(const u32x2*)(src + (size_t)j * ld);
  int frag, i0;
  if (n80) {
    const int blk = col / 80, cl = col - blk * 80;
    const int nt = cl < 64 ? 2 * (cl >> 5) + ((cl >> 2) & 1) : 4;
    i0 = cl < 64 ? 4 * ((cl >> 3) & 3) : cl - 64;
    frag = (blk * nks + ks) * 5 + nt;
  } else {
    const int blk = col / 96, cl = col - blk * 96;
    i0 = cl & 15;
    frag = (blk * nks + ks) * 6 + (cl >> 4);
  }
  const unsigned o = dst + frag * 1024 + (c.g * 16 + i0) * 16;
#pragma unroll
  for (int j = 0; j < 4; j++) {
    const unsigned sel = (j & 1) ? 0x07060302u : 0x05040100u;
    u32x4 q;
#pragma unroll
    for (int t = 0; t < 4; t++) q[t] = __builtin_amdgcn_perm(v[2 * t + 1][j >> 1], v[2 * t][j >> 1], sel);
    st16(c.rs, o + j * 16, q);
  }
}

enum { EPI_WO = 1, EPI_FF1 = 2, EPI_FF2 = 3 };
template <int EPI> struct Cfg;
template <> struct Cfg<EPI_WO>  { static constexpr int BM = 64,  BN = 80, KT = 640,  KS = 2, WM = 4, WN = 1, WK = 2, NSA = 3, NSB = 5, BB = 61440; static constexpr unsigned A = WS_O, B = WS_WO; };
template <> struct Cfg<EPI_FF1> { static constexpr int BM = 128, BN = 96, KT = 640,  KS = 1, WM = 4, WN = 2, WK = 1, NSA = 4, NSB = 8, BB = 65536; static constexpr unsigned A = WS_H, B = WS_W1; };
template <> struct Cfg<EPI_FF2> { static constexpr int BM = 64,  BN = 80, KT = 1536, KS = 2, WM = 4, WN = 1, WK = 2, NSA = 3, NSB = 5, BB = 61440; static constexpr unsigned A = WS_G, B = WS_W2; };

template <int EPI> __device__ __forceinline__ void prefetch_b(Ctx& c, int cb) {
  using C = Cfg<EPI>;
  constexpr int PB = C::KS * C::BN / 8, TOT = C::NSB * PB;
  if (c.w < 7) {
    const char* src = c.ws + C::B + (size_t)cb * (C::KT / 32) * (C::BN / 16) * 1024 + c.lane * 16;
#pragma unroll 1
    for (int q = c.w; q < TOT; q += 7) glds16<16>(src + q * 1024, c.lds + C::BB + q * 1024);
  }
}
constexpr int after_a(int s, int NKT, int NSA, int NSB, int a, int b) {
  int n = 0, j0 = 0;
  if (s < NSA) n = ((NSA < NKT ? NSA : NKT) - 1 - s) * a;
  else { n = (s - NSA + NSB < NKT) ? b : 0; j0 = s - NSA + 1; }
  for (int j = j0; j <= s - 2; j++) n += (j + NSA < NKT ? a : 0) + (j + NSB < NKT ? b : 0);
  return n;
}

template <int EPI>
__device__ __forceinline__ void gemm_phase(Ctx& c, const Params& p, int rb, int cb, u32x2 (&hres)[5]) {
  using C = Cfg<EPI>;
  constexpr int BM = C::BM, BN = C::BN, KS = C::KS, WM = C::WM, WN = C::WN, WK = C::WK, NSA = C::NSA, NSB = C::NSB;
  constexpr int MT = BM / WM / 16, NT = BN / WN / 16, NTB = BN / 16, NKS = C::KT / 32, NKT = C::KT / (64 * KS);
  constexpr int PA = KS * BM / 8, PB = KS * BN / 8, NA = PA / 8, NB = (PB + 7) / 8, KK = 2 * KS / WK;
  static_assert(PB - (NB - 1) * 8 == 4 && NSB > NSA && NSA >= 2 && NSA * PA * 1024 <= C::BB && C::BB + NSB * PB * 1024 <= LDS_BYTES, "");
  LAS char* lds = c.lds;
  const int lane = c.lane, w = c.w, r16 = c.r16, g = c.g;
  const int wm = w % WM, wn = (w / WM) % WN, wk = w / (WM * WN);
  const int row0 = rb * BM, col0 = cb * BN;
  auto ntile = [&](int n) { int v = n + (EPI == EPI_FF1 ? wm : 0); if (v >= NT) v -= NT; if (v >= NT) v -= NT; return v; };

  u32x4 e4[2];
  u32x2 e2[3];
  if (EPI == EPI_WO) {
    const bf16* xr = p.x + (size_t)(row0 + wm * 16 + r16) * D + col0;
    e4[0] = *(const u32x4*)(xr + g * 8); e4[1] = *(const u32x4*)(xr + 32 + g * 8); e2[0] = *(const u32x2*)(xr + 64 + g * 4);
  } else if (EPI == EPI_FF2) {
    const bf16* br = p.b2 + col0;
    e4[0] = *(const u32x4*)(br + g * 8); e4[1] = *(const u32x4*)(br + 32 + g * 8); e2[0] = *(const u32x2*)(br + 64 + g * 4);
  } else {
#pragma unroll
    for (int m = 0; m < 2; m++) e4[m] = __builtin_amdgcn_raw_buffer_load_b128(c.rs, WS_SP + (row0 + wm * 32 + m * 16 + r16) * 64 + g * 16, 0, 16);
#pragma unroll
    for (int n = 0; n < 3; n++) e2[n] = *(const u32x2*)(p.b1 + col0 + wn * 48 + ntile(n) * 16 + g * 4);
  }

  const char* srcA[NA];
#pragma unroll
  for (int i = 0; i < NA; i++) {
    const int pc = w * NA + i;
    srcA[i] = c.ws + C::A + (size_t)(((row0 >> 4) + pc / (2 * KS)) * NKS + pc % (2 * KS)) * 1024 + lane * 16;
  }
  const char* srcB = c.ws + C::B + (size_t)(cb * NKS * NTB + w) * 1024 + lane * 16;
  auto issue_a = [&](int t, int i) { glds16<16>(srcA[i] + t * (2 * KS * 1024), lds + (t % NSA) * (PA * 1024) + (w * NA + i) * 1024); };
  auto issue_b = [&](int t, int i) {
    if (i < NB - 1 || w < 4) glds16<16>(srcB + (t * PB + i * 8) * 1024, lds + C::BB + (t % NSB) * (PB * 1024) + (w + i * 8) * 1024);
  };
#pragma unroll
  for (int t = 0; t < NSA; t++)
#pragma unroll
    for (int i = 0; i < NA; i++) issue_a(t, i);

  f32x4 acc[MT][NT], a_cs = {0.f, 0.f, 0.f, 0.f};
#pragma unroll
  for (int m = 0; m < MT; m++)
#pragma unroll
    for (int n = 0; n < NT; n++) acc[m][n] = a_cs;
  const u32x4 ones = {0x3f803f80u, 0x3f803f80u, 0x3f803f80u, 0x3f803f80u};
  const int aoff = (wm * MT * 2 * KS + (WK > 1 ? wk : 0)) * 1024 + lane * 16;
  int boff[NT];
#pragma unroll
  for (int n = 0; n < NT; n++) boff[n] = C::BB + ((WK > 1 ? wk : 0) * NTB + wn * NT + ntile(n)) * 1024 + lane * 16;
  u32x4 af[2][KK][MT], bfr[2][KK][NT];
  constexpr int RK = MT + NT, ND = KK * RK;
  auto read1 = [&](int t, int r) {
    const int kk = r / RK, j = r % RK, bi = t & 1;
    if (j < MT) af[bi][kk][j] = *(LAS u32x4*)(lds + (t % NSA) * (PA * 1024) + aoff + (j * 2 * KS + kk * WK) * 1024);
    else bfr[bi][kk][j - MT] = *(LAS u32x4*)(lds + (t % NSB) * (PB * 1024) + boff[j - MT] + kk * WK * NTB * 1024);
  };
  auto wait_stage = [&](auto sc) {
    constexpr int s = sc.value;
    constexpr int hi = after_a(s, NKT, NSA, NSB, NA, NB), lo = after_a(s, NKT, NSA, NSB, NA, NB - 1);
    if constexpr (hi == lo) wait_vm<hi>();
    else { if (w < 4) wait_vm<hi>(); else wait_vm<lo>(); }
  };
  wait_stage(std::integral_constant<int, 0>{});
  bar();
#pragma unroll
  for (int r = 0; r < ND; r++) read1(0, r);
  static_for<0, NKT>([&](auto tc) {
    constexpr int t = tc.value, bi = t & 1;
    if constexpr (t + 1 < NKT) { wait_stage(std::integral_constant<int, t + 1>{}); bar(); }
    SB();
    constexpr int PK = MT * NT + (EPI == EPI_FF1 ? 1 : 0), NM = KK * PK, RS = NM - NM / 4, NG = NA + NB;
#pragma unroll
    for (int i = 0; i < NM; i++) {
      const int kk = i / PK, j = i % PK;
      if (j < MT * NT) acc[j / NT][j % NT] = mma16(bfr[bi][kk][j % NT], af[bi][kk][j / NT], acc[j / NT][j % NT]);
      else a_cs = mma16(ones, bfr[bi][kk][0], a_cs);
      if (t + 1 < NKT) {
#pragma unroll
        for (int r = 0; r < ND; r++) if (r * RS / ND == i) read1(t + 1, r);
      }
#pragma unroll
      for (int q = 0; q < NG; q++)
        if ((2 * q + 1) * NM / (2 * NG) == i) {
          if (q < NA) { if (t + NSA < NKT) issue_a(t + NSA, q); }
          else if (t + NSB < NKT) issue_b(t + NSB, q - NA);
        }
      SB();
    }
  });
  STAMP(EPI * 3 + 6);
  bar();
  if constexpr (EPI == EPI_FF1) {
    LAS float* st_cs = (LAS float*)lds;
    if (g == 0 && wm < NT) st_cs[(wn * NT + wm) * 16 + r16] = a_cs[0];
    float mean[2], rs[2];
#pragma unroll
    for (int m = 0; m < 2; m++) {
      const f32x4 q = __builtin_bit_cast(f32x4, e4[m]);
      float s1 = q[0] + q[2], s2 = q[1] + q[3];
      s1 += __shfl_xor(s1, 16); s2 += __shfl_xor(s2, 16);
      s1 += __shfl_xor(s1, 32); s2 += __shfl_xor(s2, 32);
      mean[m] = s1 * (1.f / D);
      rs[m] = __builtin_amdgcn_rsqf(fmaxf(s2 * (1.f / D) - mean[m] * mean[m], 0.f) + 1e-5f);
    }
    bar();
#pragma unroll
    for (int n = 0; n < NT; n++) {
      const int lc = (wn * NT + ntile(n)) * 16;
      const f32x4 c4 = *(LAS f32x4*)(st_cs + lc + g * 4);
      u32x2 o[2];
#pragma unroll
      for (int m = 0; m < 2; m++) {
        const float v0 = fmaxf(rs[m] * (acc[m][n][0] - mean[m] * c4[0]) + bflo(e2[n][0]), 0.f);
        const float v1 = fmaxf(rs[m] * (acc[m][n][1] - mean[m] * c4[1]) + bfhi(e2[n][0]), 0.f);
        const float v2 = fmaxf(rs[m] * (acc[m][n][2] - mean[m] * c4[2]) + bflo(e2[n][1]), 0.f);
        const float v3 = fmaxf(rs[m] * (acc[m][n][3] - mean[m] * c4[3]) + bfhi(e2[n][1]), 0.f);
        o[m] = (u32x2){pk2(v0 * v0, v1 * v1), pk2(v2 * v2, v3 * v3)};
      }
      const auto s0 = __builtin_amdgcn_permlane16_swap(o[0][0], o[1][0], false, false);
      const auto s1 = __builtin_amdgcn_permlane16_swap(o[0][1], o[1][1], false, false);
      const int c8 = ((col0 + lc) >> 3) + (g >> 1), row16 = ((row0 + wm * 32) >> 4) + (g & 1);
      st16(c.rs, WS_G + ((row16 * 48 + (c8 >> 2)) * 64 + (c8 & 3) * 16 + r16) * 16, (u32x4){s0[0], s1[0], s0[1], s1[1]});
    }
  } else {
    LAS f32x4* red = (LAS f32x4*)lds + wm * NT * 64 + lane;
    if (wk == 1) {
#pragma unroll
      for (int n = 0; n < NT; n++) red[n * 64] = acc[0][n];
    }
    bar();
    if (wk == 0) {
      const u32x2 ev[5] = {{e4[0][0], e4[0][1]}, {e4[0][2], e4[0][3]}, {e4[1][0], e4[1][1]}, {e4[1][2], e4[1][3]}, e2[0]};
      u32x2 o[5];
#pragma unroll
      for (int n = 0; n < NT; n++) {
        f32x4 v = acc[0][n] + red[n * 64];
        v[0] += bflo(ev[n][0]); v[1] += bfhi(ev[n][0]); v[2] += bflo(ev[n][1]); v[3] += bfhi(ev[n][1]);
        if (EPI == EPI_FF2) { v[0] += bflo(hres[n][0]); v[1] += bfhi(hres[n][0]); v[2] += bflo(hres[n][1]); v[3] += bfhi(hres[n][1]); }
        o[n] = (u32x2){pk2(v[0], v[1]), pk2(v[2], v[3])};
      }
      const int row = row0 + wm * 16 + r16;
      if (EPI == EPI_WO) {
        const int row16 = row >> 4;
        float s1 = 0.f, s2 = 0.f;
#pragma unroll
        for (int n = 0; n < NT; n++) {
          hres[n] = o[n];
          const float a0 = bflo(o[n][0]), a1 = bfhi(o[n][0]), a2 = bflo(o[n][1]), a3 = bfhi(o[n][1]);
          s1 += (a0 + a1) + (a2 + a3);
          s2 += (a0 * a0 + a1 * a1) + (a2 * a2 + a3 * a3);
        }
#pragma unroll
        for (int q = 0; q < 2; q++) {
          const int c8 = cb * 10 + q * 4 + g;
          st16(c.rs, WS_H + ((row16 * 20 + (c8 >> 2)) * 64 + (c8 & 3) * 16 + r16) * 16, (u32x4){o[2 * q][0], o[2 * q][1], o[2 * q + 1][0], o[2 * q + 1][1]});
        }
        const int c8 = cb * 10 + 8 + (g >> 1);
        st8(c.rs, WS_H + ((row16 * 20 + (c8 >> 2)) * 64 + (c8 & 3) * 16 + r16) * 16 + (g & 1) * 8, o[4]);
        s1 += __shfl_xor(s1, 16); s2 += __shfl_xor(s2, 16);
        s1 += __shfl_xor(s1, 32); s2 += __shfl_xor(s2, 32);
        if (g == 0) st8(c.rs, WS_SP + row * 64 + cb * 8, (u32x2){__builtin_bit_cast(unsigned, s1), __builtin_bit_cast(unsigned, s2)});
      } else {
        bf16* op = p.out + (size_t)row * D + col0;
        *(u32x4*)(op + g * 8) = (u32x4){o[0][0], o[0][1], o[1][0], o[1][1]};
        *(u32x4*)(op + 32 + g * 8) = (u32x4){o[2][0], o[2][1], o[3][0], o[3][1]};
        *(u32x2*)(op + 64 + g * 4) = o[4];
      }
    }
  }
}

__device__ __forceinline__ f32x16 mma32(u32x4 a, u32x4 b, f32x16 c) {
  return __builtin_amdgcn_mfma_f32_32x32x16_bf16(__builtin_bit_cast(bf16x8, a), __builtin_bit_cast(bf16x8, b), c, 0, 0, 0);
}
__device__ __forceinline__ f32x16 mma32h(u32x2 a, u32x2 b, f32x16 c) {
  return __builtin_amdgcn_mfma_f32_32x32x8bf16_1k(__builtin_bit_cast(s16x4, a), __builtin_bit_cast(s16x4, b), c, 0, 0, 0);
}
__device__ __forceinline__ float max16(const f32x16& s) {
  float m = fmaxf(fmaxf(s[0], s[1]), s[2]);
#pragma unroll
  for (int i = 3; i < 15; i += 2) m = fmaxf(fmaxf(m, s[i]), s[i + 1]);
  return fmaxf(m, s[15]);
}
__device__ __forceinline__ u32x2 set_shift(u32x2 qhi, float m, int hh) {
  const float a = bflo(pk2(m, 0.f));
  const float r = m - a;
  const float b = bflo(pk2(r, 0.f));
  const u32x2 e = {pk2(-a, -b), pk2(b - r, 0.f)};
  return hh ? e : qhi;
}
__device__ __forceinline__ u32x2 exp4(const f32x16& s, int i) {
  const float e0 = __builtin_amdgcn_exp2f(s[i]), e1 = __builtin_amdgcn_exp2f(s[i + 1]);
  const float e2 = __builtin_amdgcn_exp2f(s[i + 2]), e3 = __builtin_amdgcn_exp2f(s[i + 3]);
  return (u32x2){pk2(e0, e1), pk2(e2, e3)};
}

__device__ __forceinline__ void attn_phase(Ctx& c, int head, int qb, int wo_cb) {
  LAS char* lds = c.lds;
  const int lane = c.lane, w = c.w;
  const int r = lane & 31, hh = lane >> 5;
  const int qrow = qb * 256 + w * 32 + r;
  const char* kvg = c.ws + WS_KV + (size_t)head * KV_HEAD;
  const char* qp = c.ws + WS_Q + (size_t)(head * 5 * S + qrow) * 8;
  u32x2 qa, qc, qhi0;
  asm volatile("global_load_dwordx2 %0, %1, off sc1" : "=v"(qa) : "v"(qp + (size_t)(2 * hh) * S * 8));
  asm volatile("global_load_dwordx2 %0, %1, off sc1" : "=v"(qc) : "v"(qp + (size_t)(2 * hh + 1) * S * 8));
  asm volatile("global_load_dwordx2 %0, %1, off sc1" : "=v"(qhi0) : "v"(qp + (size_t)4 * S * 8));
  auto issue = [&](int s) {
    const char* gsrc = kvg + (size_t)s * AT_STAGE + lane * 16;
    LAS char* sb = lds + (s % 3) * AT_STAGE;
#pragma unroll
    for (int i = 0; i < 5; i++) glds16<16>(gsrc + (w + 8 * i) * 1024, sb + (w + 8 * i) * 1024);
    if (w < 6) glds16<16>(gsrc + (w + 40) * 1024, sb + (w + 40) * 1024);
  };
  issue(0); issue(1); issue(2);
  if (w < 6) wait_vm<12>(); else wait_vm<10>();
  asm volatile("" : "+v"(qa), "+v"(qc), "+v"(qhi0));
  const u32x4 qlo = {qa[0], qa[1], qc[0], qc[1]};
  bar();
  STAMP(5);

  const int koff = hh * 512 + r * 8, khoff = 1024 + lane * 8, voff = 1536 + hh * 352 + (r <= 20 ? r : 21) * 16;
  int koff2 = koff + 256;
  asm volatile("" : "+v"(koff2));
  auto ldk = [&](LAS char* t) { const u32x2 a = *(LAS u32x2*)(t + koff), b = *(LAS u32x2*)(t + koff2); return (u32x4){a[0], a[1], b[0], b[1]}; };
  const u32x2 qz = hh ? (u32x2){0u, 0u} : qhi0;
  const f32x16 zero = {0.f, 0.f, 0.f, 0.f, 0.f, 0.f, 0.f, 0.f, 0.f, 0.f, 0.f, 0.f, 0.f, 0.f, 0.f, 0.f};
  u32x2 qhi;
  f32x16 sc;
  {
    const u32x4 k0 = ldk(lds);
    const u32x2 kh0 = *(LAS u32x2*)(lds + khoff);
    const f32x16 s0 = mma32(k0, qlo, zero);
    float m = max16(mma32h(kh0, qz, s0));
    m = fmaxf(m, __shfl_xor(m, 32));
    qhi = set_shift(qhi0, m, hh);
    sc = mma32h(kh0, qhi, s0);
  }
  f32x16 o = zero;
  u32x4 klo = ldk(lds + KV_TILE);
  u32x2 khi = *(LAS u32x2*)(lds + KV_TILE + khoff);
  u32x4 v0 = *(LAS u32x4*)(lds + voff), v1 = *(LAS u32x4*)(lds + voff + 704);
  u32x4 v1p = v1, p1p = {0u, 0u, 0u, 0u};
  u32x4 fr0 = p1p, fr1 = p1p;
#pragma unroll 1
  for (int s = 0; s < 4; s++) {
    LAS char* cb = lds + (s == 3 ? 0 : s) * AT_STAGE;
    LAS char* nb = lds + (s >= 2 ? s - 2 : s + 1) * AT_STAGE;
#pragma unroll
    for (int t = 0; t < 16; t++) {
      if (t == 14 && s < 3) {
        if (s == 0) { if (w < 6) wait_vm<6>(); else wait_vm<5>(); } else wait_vm<0>();
        if (s == 2 && w == 7) {
          asm volatile("" : "+v"(fr0), "+v"(fr1));
          const unsigned lo = (unsigned)c.token, hi = (unsigned)(c.token >> 32);
          if (!__all(fr0[0] == lo && fr0[1] == hi && fr0[2] == lo && fr0[3] == hi && fr1[0] == lo && fr1[1] == hi && fr1[2] == lo && fr1[3] == hi)) {
            spin_flags(c, F_RT + lane, 64, F_RT + 64 + lane, 64);
            spin_flags(c, F_RT + 128 + lane, 64, F_RT + 192 + lane, 64);
          }
        }
        rawbar();
        if (s == 0 && c.tid == 0)
          __hip_atomic_store((u64*)(c.ws + WS_FLAG) + F_RT + blockIdx.x, c.token, __ATOMIC_RELAXED, __HIP_MEMORY_SCOPE_AGENT);
        if (s == 1) {
          issue(3);
          if (w == 7) {
            const char* fp = c.ws + WS_FLAG + (F_RT + lane * 4) * 8;
            asm volatile("global_load_dwordx4 %0, %1, off sc1" : "=v"(fr0) : "v"(fp));
            asm volatile("global_load_dwordx4 %0, %1, off offset:16 sc1" : "=v"(fr1) : "v"(fp));
          }
        }
      }
      if (t == 2 && s == 3) { rawbar(); prefetch_b<EPI_WO>(c, wo_cb); }
      LAS char* pk = (t + 2 < 16 ? cb + (t + 2) * KV_TILE : nb + (t - 14) * KV_TILE);
      LAS char* pv = (t + 1 < 16 ? cb + (t + 1) * KV_TILE : nb);
      SB();
      f32x16 sn = mma32(klo, qlo, zero);
      SB();
      const u32x2 pa = exp4(sc, 0);
      SB();
      sn = mma32h(khi, qhi, sn);
      SB();
      klo = ldk(pk);
      khi = *(LAS u32x2*)(pk + khoff);
      const u32x2 pb = exp4(sc, 4);
      SB();
      o = mma32(v1p, p1p, o);
      SB();
      const u32x2 pc = exp4(sc, 8);
      SB();
      o = mma32(v0, (u32x4){pa[0], pa[1], pb[0], pb[1]}, o);
      SB();
      v1p = v1;
      v0 = *(LAS u32x4*)(pv + voff);
      v1 = *(LAS u32x4*)(pv + voff + 704);
      const u32x2 pd = exp4(sc, 12);
      p1p = (u32x4){pc[0], pc[1], pd[0], pd[1]};
      sc = sn;
    }
  }
  o = mma32(v1p, p1p, o);
  STAMP(6);
  float l = __shfl(o[8], r + 32);
  if (!__all(l < 1.8446744e19f)) {
    const unsigned kb = WS_KV + head * KV_HEAD;
    auto gk = [&](int t) {
      const u32x2 a = __builtin_amdgcn_raw_buffer_load_b64(c.rs, kb + t * KV_TILE + koff, 0, 16);
      const u32x2 b = __builtin_amdgcn_raw_buffer_load_b64(c.rs, kb + t * KV_TILE + koff + 256, 0, 16);
      return mma32((u32x4){a[0], a[1], b[0], b[1]}, qlo, zero);
    };
    float m = -__builtin_inff();
#pragma unroll 1
    for (int t = 0; t < 64; t++)
      m = fmaxf(m, max16(mma32h(__builtin_amdgcn_raw_buffer_load_b64(c.rs, kb + t * KV_TILE + khoff, 0, 16), qz, gk(t))));
    m = fmaxf(m, __shfl_xor(m, 32));
    qhi = set_shift(qhi0, m, hh);
    o = zero;
#pragma unroll 1
    for (int t = 0; t < 64; t++) {
      const f32x16 s2 = mma32h(__builtin_amdgcn_raw_buffer_load_b64(c.rs, kb + t * KV_TILE + khoff, 0, 16), qhi, gk(t));
      const u32x2 pa = exp4(s2, 0), pb = exp4(s2, 4), pc = exp4(s2, 8), pd = exp4(s2, 12);
      o = mma32(__builtin_amdgcn_raw_buffer_load_b128(c.rs, kb + t * KV_TILE + voff, 0, 16), (u32x4){pa[0], pa[1], pb[0], pb[1]}, o);
      o = mma32(__builtin_amdgcn_raw_buffer_load_b128(c.rs, kb + t * KV_TILE + voff + 704, 0, 16), (u32x4){pc[0], pc[1], pd[0], pd[1]}, o);
    }
    l = __shfl(o[8], r + 32);
  }
  const float inv = 1.f / l;
#pragma unroll
  for (int j = 0; j < 3; j++) {
    const int col = head * DH + j * 8 + hh * 4;
    if (j < 2 || hh == 0)
      st8(c.rs, WS_O + (((qrow >> 4) * 20 + (col >> 5)) * 64 + ((col >> 3) & 3) * 16 + (qrow & 15)) * 16 + (col & 4) * 2,
          (u32x2){pk2(o[4 * j] * inv, o[4 * j + 1] * inv), pk2(o[4 * j + 2] * inv, o[4 * j + 3] * inv)});
  }
}

__global__ __launch_bounds__(512, 2) void k_block(Params p) {
  extern __shared__ __attribute__((aligned(16))) char smem[];
  Ctx c;
  c.tid = threadIdx.x; c.lane = c.tid & 63; c.w = __builtin_amdgcn_readfirstlane(c.tid >> 6);
  c.r16 = c.lane & 15; c.g = c.lane >> 4;
  c.lds = (LAS char*)smem; c.ws = p.ws;
  c.rs = __builtin_amdgcn_make_buffer_rsrc(p.ws, 0, WS_END, 0x00020000);
  STAMP(0);
  const u64 epoch = __hip_atomic_load((const u64*)(p.ws + WS_FLAG) + F_EPOCH, __ATOMIC_RELAXED, __HIP_MEMORY_SCOPE_AGENT);
  const int b = blockIdx.x, xcd = b & 7, i = b >> 3;
  {
    const int rb = (xcd & 3) * 4 + (i & 3), cb = (xcd >> 2) * 8 + (i >> 2);
    if (cb < 10) qkv_phase<false>(c, p, rb, cb);
    else if (cb < 15) qkv_phase<true>(c, p, rb, cb);
    u64 z = (epoch + 0x9e3779b97f4a7c15ull) * 0xbf58476d1ce4e5b9ull;
    c.token = z ^ (z >> 31);
    if (cb < 15) publish(c, F_QKV + cb * 16 + rb);
    STAMP(3);
  }
  if (c.w < 5 && b * 5 + c.w < 1160) retile(c, p, b * 5 + c.w);
  const int head = xcd * 4 + (i & 3), qb = i >> 2;
  {
    const int c0 = head * 20 / 128, c1 = (head * 20 + 19) / 128, j = c.lane >> 4;
    await(c, F_QKV + (((j & 1) ? c1 : c0) + (j >> 1) * 5) * 16 + c.r16, 64, F_QKV + (((j & 1) ? c1 : c0) + 10) * 16 + c.r16, 32);
  }
  STAMP(4);
  const int rb64 = xcd * 4 + (i & 3), cb80 = i >> 2, rb128 = xcd * 2 + (i & 1), cb96 = i >> 1;
  attn_phase(c, head, qb, cb80);
  publish(c, F_ATT + qb * 32 + head);
  STAMP(7);
  await(c, F_ATT + xcd * 32 + c.lane, 32, 0, 0);
  STAMP(8);
  u32x2 hres[5];
  gemm_phase<EPI_WO>(c, p, rb64, cb80, hres);
  publish(c, F_WO + rb64 * 8 + cb80);
  STAMP(10);
  prefetch_b<EPI_FF1>(c, cb96);
  await(c, F_WO + rb128 * 16 + c.lane, 16, 0, 0);
  STAMP(11);
  gemm_phase<EPI_FF1>(c, p, rb128, cb96, hres);
  publish(c, F_FF1 + rb128 * 16 + cb96);
  STAMP(13);
  prefetch_b<EPI_FF2>(c, cb80);
  await(c, F_FF1 + (rb64 >> 1) * 16 + c.lane, 16, 0, 0);
  STAMP(14);
  gemm_phase<EPI_FF2>(c, p, rb64, cb80, hres);
  if (b == 0 && c.tid == 0) __hip_atomic_store((u64*)(p.ws + WS_FLAG) + F_EPOCH, epoch + 1, __ATOMIC_RELAXED, __HIP_MEMORY_SCOPE_AGENT);
}

extern "C" void kernel_launch(void* const* d_in, const int* in_sizes, int n_in, void* d_out, int out_size, void* d_ws,
                              size_t ws_size, hipStream_t stream) {
  Params p;
  p.x = (const bf16*)d_in[0]; p.wqkv = (const bf16*)d_in[1]; p.wo = (const bf16*)d_in[2];
  p.w1 = (const bf16*)d_in[4]; p.b1 = (const bf16*)d_in[5]; p.w2 = (const bf16*)d_in[6]; p.b2 = (const bf16*)d_in[7];
  p.out = (bf16*)d_out; p.ws = (char*)d_ws;
  (void)hipFuncSetAttribute((const void*)k_block, hipFuncAttributeMaxDynamicSharedMemorySize, LDS_BYTES);
  k_block<<<256, 512, LDS_BYTES, stream>>>(p);
}
```

```cpp
#include <hip/hip_runtime.h>
#include <hip/hip_bf16.h>
#include <stdint.h>
#include <type_traits>

#define LAS __attribute__((address_space(3)))
#define GAS __attribute__((address_space(1)))
typedef __bf16 bf16;
typedef unsigned long long u64;
typedef __bf16 bf16x2 __attribute__((ext_vector_type(2)));
typedef __bf16 bf16x8 __attribute__((ext_vector_type(8)));
typedef short s16x4 __attribute__((ext_vector_type(4)));
typedef float f32x2 __attribute__((ext_vector_type(2)));
typedef float f32x4 __attribute__((ext_vector_type(4)));
typedef float f32x16 __attribute__((ext_vector_type(16)));
typedef unsigned u32x2 __attribute__((ext_vector_type(2)));
typedef unsigned u32x4 __attribute__((ext_vector_type(4)));
typedef __amdgpu_buffer_rsrc_t rsrc_t;

constexpr int S = 2048, D = 640, NH = 32, DH = 20, FF = 1536;
constexpr int KV_TILE = 2944;
constexpr int KV_HEAD = 64 * KV_TILE;
constexpr int AT_STAGE = 16 * KV_TILE;
constexpr int LDS_BYTES = 163840;
constexpr unsigned WS_FLAG = 0;
constexpr unsigned F_QKV = 0, F_ATT = 256, F_WO = 512, F_FF1 = 768, F_RT = 1024, F_EPOCH = 1536;
constexpr unsigned WS_Q = 16384;
constexpr unsigned WS_KV = WS_Q + NH * S * 40;
constexpr unsigned WS_O = WS_KV + NH * KV_HEAD;
constexpr unsigned WS_H = WS_O + S * D * 2;
constexpr unsigned WS_G = WS_H + S * D * 2;
constexpr unsigned WS_WO = WS_G + S * FF * 2;
constexpr unsigned WS_W1 = WS_WO + D * D * 2;
constexpr unsigned WS_W2 = WS_W1 + D * FF * 2;
constexpr unsigned WS_SP = WS_W2 + D * FF * 2;
constexpr unsigned WS_END = WS_SP + S * 64;

#define SB() __builtin_amdgcn_sched_barrier(0)
template <int N> __device__ __forceinline__ void wait_vm() { asm volatile("s_waitcnt vmcnt(%0)" ::"n"(N) : "memory"); }
__device__ __forceinline__ void bar() { asm volatile("s_waitcnt lgkmcnt(0)" ::: "memory"); __builtin_amdgcn_s_barrier(); asm volatile("" ::: "memory"); }
__device__ __forceinline__ void rawbar() { asm volatile("" ::: "memory"); __builtin_amdgcn_s_barrier(); asm volatile("" ::: "memory"); }
template <int AUX> __device__ __forceinline__ void glds16(const void* g, LAS char* l) {
  __builtin_amdgcn_global_load_lds((const GAS void*)g, (LAS void*)l, 16, 0, AUX);
}
__device__ __forceinline__ void blds16(rsrc_t r, unsigned voff, unsigned soff, LAS char* l) {
  __builtin_amdgcn_raw_ptr_buffer_load_lds(r, (LAS void*)l, 16, voff, soff, 0, 16);
}
__device__ __forceinline__ void st16(rsrc_t r, unsigned off, u32x4 v) { __builtin_amdgcn_raw_buffer_store_b128(v, r, off, 0, 16); }
__device__ __forceinline__ void st8(rsrc_t r, unsigned off, u32x2 v) { __builtin_amdgcn_raw_buffer_store_b64(v, r, off, 0, 16); }
__device__ __forceinline__ unsigned pk2(float a, float b) {
  f32x2 f = {a, b};
  return __builtin_bit_cast(unsigned, __builtin_convertvector(f, bf16x2));
}
__device__ __forceinline__ float bflo(unsigned u) { return __builtin_bit_cast(float, u << 16); }
__device__ __forceinline__ float bfhi(unsigned u) { return __builtin_bit_cast(float, u & 0xffff0000u); }
__device__ __forceinline__ f32x4 mma16(u32x4 a, u32x4 b, f32x4 c) {
  return __builtin_amdgcn_mfma_f32_16x16x32_bf16(__builtin_bit_cast(bf16x8, a), __builtin_bit_cast(bf16x8, b), c, 0, 0, 0);
}
template <int I, int N, class F> __device__ __forceinline__ void static_for(F&& f) {
  if constexpr (I < N) { f(std::integral_constant<int, I>{}); static_for<I + 1, N>(f); }
}

struct Params {
  const bf16 *x, *wqkv, *wo, *w1, *b1, *w2, *b2;
  bf16* out;
  char* ws;
};
struct Ctx {
  int tid, lane, w, r16, g;
  LAS char* lds;
  char* ws;
  rsrc_t rs;
  u32x4 rd;
  u64 token;
};
#define CYC(i) do {} while (0)
#define CYC3(i) do {} while (0)
#define STAMP(i) do {} while (0)

__device__ __forceinline__ void publish(Ctx& c, unsigned flag) {
  wait_vm<0>();
  bar();
  if (c.tid == 0) __hip_atomic_store((u64*)(c.ws + WS_FLAG) + flag, c.token, __ATOMIC_RELAXED, __HIP_MEMORY_SCOPE_AGENT);
}
__device__ __forceinline__ void spin_flags(Ctx& c, unsigned idx0, int n0, unsigned idx1, int n1) {
  const u64* f = (const u64*)(c.ws + WS_FLAG);
  for (int it = 0; it < (1 << 20); it++) {
    u64 a = c.token, b = c.token;
    if (c.lane < n0) a = __hip_atomic_load(f + idx0, __ATOMIC_RELAXED, __HIP_MEMORY_SCOPE_AGENT);
    if (c.lane < n1) b = __hip_atomic_load(f + idx1, __ATOMIC_RELAXED, __HIP_MEMORY_SCOPE_AGENT);
    if (__all(a == c.token && b == c.token)) break;
    __builtin_amdgcn_s_sleep(1);
  }
}
__device__ __forceinline__ void await(Ctx& c, unsigned idx0, int n0, unsigned idx1, int n1) {
  if (c.w == 7) spin_flags(c, idx0, n0, idx1, n1);
  rawbar();
}

template <bool ISV>
__device__ __forceinline__ void qkv_phase(Ctx& c, const Params& p, int rb, int cb) {
  constexpr int NS = 4, NKT = 10, STAGE = 32768, LDB = 3 * D;
  LAS char* lds = c.lds;
  LAS float* st_mean = (LAS float*)(lds + NS * STAGE);
  LAS float* st_rstd = st_mean + 128;
  LAS float* st_cs = st_rstd + 128;
  const int lane = c.lane, w = c.w, r16 = c.r16, g = c.g;
  const int wm = w & 3, wn = w >> 2;
  const int row0 = rb * 128, col0 = cb * 128;
  const int rot = (rb + cb) % NKT;

  const char* srcA[2];
  const char* srcB[2];
#pragma unroll
  for (int i = 0; i < 2; i++) {
    const int pc = w + 8 * i;
    {
      const int lr = pc * 8 + (lane >> 3), rho = lr & 31;
      const int gr = ISV ? (16 * ((rho >> 3) & 1) + 8 * (rho >> 4) + (rho & 7)) : (2 * (rho & 15) + (rho >> 4));
      const int ch = (lane & 7) ^ ((lr >> 1) & 7);
      srcA[i] = (const char*)(p.x + (size_t)(row0 + (lr & ~31) + gr) * D + ch * 8);
    }
    {
      const int row = lane >> 3, f = ((row >> 1) & 1) | ((w & 1) << 1);
      const int ch = (lane & 7) ^ (f << 1);
      srcB[i] = (const char*)(p.wqkv + (size_t)(w * 8 + row) * LDB + col0 + i * 64 + ch * 8);
    }
  }
  auto issue = [&](int t, int i) {
    int kt = t + rot;
    if (kt >= NKT) kt -= NKT;
    LAS char* sb = lds + (t % NS) * STAGE;
    if (i < 2) glds16<0>(srcA[i] + kt * 128, sb + (w + 8 * i) * 1024);
    else glds16<0>(srcB[i - 2] + (size_t)kt * (64 * LDB * 2), sb + 16384 + (w + 8 * (i - 2)) * 1024);
  };
#pragma unroll
  for (int t = 0; t < NS; t++)
#pragma unroll
    for (int i = 0; i < 4; i++) issue(t, i);

  int aoff[2], boff[4];
#pragma unroll
  for (int m = 0; m < 2; m++) {
    const int row = wm * 32 + (m ^ wn) * 16 + r16;
    aoff[m] = row * 128 + ((g ^ ((row >> 1) & 7)) << 4);
  }
#pragma unroll
  for (int n = 0; n < 4; n++) {
    const int q = (lane >> 2) & 3, pp = lane & 3, f = ((q >> 1) & 1) | ((g & 1) << 1);
    const int nt = (n + wm) & 3;
    boff[n] = 16384 + wn * 8192 + (g * 8 + q) * 128 + (((nt * 2 + (pp >> 1)) ^ (f << 1)) << 4) + (pp & 1) * 8;
  }
  f32x4 acc[2][4], a_s1 = {0.f, 0.f, 0.f, 0.f}, a_s2 = a_s1, a_cs = a_s1;
#pragma unroll
  for (int m = 0; m < 2; m++)
#pragma unroll
    for (int n = 0; n < 4; n++) acc[m][n] = a_s1;
  const u32x4 ones = {0x3f803f80u, 0x3f803f80u, 0x3f803f80u, 0x3f803f80u};
  u32x4 af[2][2][2];
  u32x2 bq[2][2][4][2];
  auto read1 = [&](int t, int r) {
    LAS char* sb = lds + (t % NS) * STAGE;
    const int bi = t & 1;
    if (r < 8) {
      const int kk = r >> 2, n = r & 3;
      const unsigned ad = (unsigned)(size_t)(sb + boff[n]);
      if (kk == 0) asm volatile("ds_read_b64_tr_b16 %0, %2\n\tds_read_b64_tr_b16 %1, %2 offset:512"
                                : "=&v"(bq[bi][0][n][0]), "=&v"(bq[bi][0][n][1]) : "v"(ad));
      else asm volatile("ds_read_b64_tr_b16 %0, %2 offset:4096\n\tds_read_b64_tr_b16 %1, %2 offset:4608"
                        : "=&v"(bq[bi][1][n][0]), "=&v"(bq[bi][1][n][1]) : "v"(ad));
    } else {
      const int kk = (r - 8) >> 1, m = r & 1;
      af[bi][kk][m] = *(LAS u32x4*)(sb + (aoff[m] ^ (kk << 6)));
    }
  };
  wait_vm<12>();
  bar();
  STAMP(1);
#pragma unroll
  for (int r = 0; r < 12; r++) read1(0, r);
  static_for<0, NKT>([&](auto tc) {
    constexpr int t = tc.value, bi = t & 1;
    if constexpr (t + 1 < NKT) wait_vm<4 * ((NKT - 2 - t) < (NS - 2) ? (NKT - 2 - t) : (NS - 2))>();
    asm volatile("s_waitcnt lgkmcnt(0)"
                 : "+v"(bq[bi][0][0][0]), "+v"(bq[bi][0][0][1]), "+v"(bq[bi][1][0][0]), "+v"(bq[bi][1][0][1]),
                   "+v"(bq[bi][0][1][0]), "+v"(bq[bi][0][1][1]), "+v"(bq[bi][1][1][0]), "+v"(bq[bi][1][1][1]),
                   "+v"(bq[bi][0][2][0]), "+v"(bq[bi][0][2][1]), "+v"(bq[bi][1][2][0]), "+v"(bq[bi][1][2][1]),
                   "+v"(bq[bi][0][3][0]), "+v"(bq[bi][0][3][1]), "+v"(bq[bi][1][3][0]), "+v"(bq[bi][1][3][1]));
    if constexpr (t + 1 < NKT) bar();
    SB();
#pragma unroll
    for (int i = 0; i < 22; i++) {
      const int kk = i / 11, j = i % 11;
      if (j < 8) {
        const int m = j >> 2, n = j & 3;
        const u32x4 bv = {bq[bi][kk][n][0][0], bq[bi][kk][n][0][1], bq[bi][kk][n][1][0], bq[bi][kk][n][1][1]};
        acc[m][n] = ISV ? mma16(af[bi][kk][m], bv, acc[m][n]) : mma16(bv, af[bi][kk][m], acc[m][n]);
      } else if (j == 8) {
        a_s1 = mma16(ones, af[bi][kk][0], a_s1);
      } else if (j == 9) {
        a_s2 = mma16(af[bi][kk][0], af[bi][kk][0], a_s2);
      } else {
        const u32x4 bv = {bq[bi][kk][0][0][0], bq[bi][kk][0][0][1], bq[bi][kk][0][1][0], bq[bi][kk][0][1][1]};
        a_cs = mma16(ones, bv, a_cs);
      }
      if (t + 1 < NKT && i < 18 && (i % 3) != 2) {
        const int r = (i / 3) * 2 + (i % 3);
        read1(t + 1, r < 4 ? r : (r < 6 ? r + 4 : (r < 10 ? r - 2 : r)));
      }
      if (t + NS < NKT && (i % 5) == 2 && i / 5 < 4) issue(t + NS, i / 5);
      SB();
    }
  });
  STAMP(2);
  {
    const float mean = a_s1[0] * (1.f / D);
    const int e = r16 & 3;
    const float s2 = e == 0 ? a_s2[0] : (e == 1 ? a_s2[1] : (e == 2 ? a_s2[2] : a_s2[3]));
    const float var = fmaxf(s2 * (1.f / D) - mean * mean, 0.f);
    if (g == (r16 >> 2)) {
      st_mean[wm * 32 + wn * 16 + r16] = mean;
      st_rstd[wm * 32 + wn * 16 + r16] = __builtin_amdgcn_rsqf(var + 1e-5f);
      st_cs[(wn * 4 + wm) * 16 + r16] = a_cs[0];
    }
    bar();
  }
  const int tile = (row0 >> 5) + wm;
  if (!ISV) {
    const bool isq = cb < 5;
    float mean[2], rs[2];
#pragma unroll
    for (int m = 0; m < 2; m++) {
      mean[m] = st_mean[wm * 32 + (m ^ wn) * 16 + r16];
      rs[m] = st_rstd[wm * 32 + (m ^ wn) * 16 + r16] * (isq ? 0.22360679774997896f * 1.4426950408889634f : 1.f);
    }
#pragma unroll
    for (int n = 0; n < 4; n++) {
      const int lc = (wn * 4 + ((n + wm) & 3)) * 16 + g * 4;
      const f32x4 c4 = *(LAS f32x4*)(st_cs + lc);
      u32x2 o[2];
#pragma unroll
      for (int m = 0; m < 2; m++) {
        o[m][0] = pk2(rs[m] * (acc[m][n][0] - mean[m] * c4[0]), rs[m] * (acc[m][n][1] - mean[m] * c4[1]));
        o[m][1] = pk2(rs[m] * (acc[m][n][2] - mean[m] * c4[2]), rs[m] * (acc[m][n][3] - mean[m] * c4[3]));
      }
      const u32x4 v = wn ? (u32x4){o[1][0], o[1][1], o[0][0], o[0][1]} : (u32x4){o[0][0], o[0][1], o[1][0], o[1][1]};
      const unsigned cc = col0 + lc - (isq ? 0 : D);
      if (isq) {
        st16(c.rs, WS_Q + ((cc >> 2) * S + tile * 32 + 2 * r16) * 8, v);
      } else {
        const unsigned head = cc / 20u, d0 = cc - head * 20u;
        const unsigned t = WS_KV + head * KV_HEAD + tile * KV_TILE + r16 * 16;
        st16(c.rs, t + d0 * 64, v);
        if (d0 == 16) st16(c.rs, t + 1280, (u32x4){0x3f803f80u, 0x00003f80u, 0x3f803f80u, 0x00003f80u});
      }
    }
  } else {
    f32x4 mean[2], rs[2];
#pragma unroll
    for (int m = 0; m < 2; m++) {
      mean[m] = *(LAS f32x4*)(st_mean + wm * 32 + (m ^ wn) * 16 + g * 4);
      rs[m] = *(LAS f32x4*)(st_rstd + wm * 32 + (m ^ wn) * 16 + g * 4);
    }
#pragma unroll
    for (int n = 0; n < 4; n++) {
      const int lc = (wn * 4 + ((n + wm) & 3)) * 16 + r16;
      const float cs = st_cs[lc];
      u32x2 o[2];
#pragma unroll
      for (int m = 0; m < 2; m++) {
        o[m][0] = pk2(rs[m][0] * (acc[m][n][0] - mean[m][0] * cs), rs[m][1] * (acc[m][n][1] - mean[m][1] * cs));
        o[m][1] = pk2(rs[m][2] * (acc[m][n][2] - mean[m][2] * cs), rs[m][3] * (acc[m][n][3] - mean[m][3] * cs));
      }
      const u32x4 v = wn ? (u32x4){o[1][0], o[1][1], o[0][0], o[0][1]} : (u32x4){o[0][0], o[0][1], o[1][0], o[1][1]};
      const unsigned cc = col0 + lc - 2 * D;
      const unsigned head = cc / 20u, d = cc - head * 20u;
      const unsigned t = WS_KV + head * KV_HEAD + tile * KV_TILE + 1536 + g * 352;
      st16(c.rs, t + ((head & 1) ? (d < 4 ? d + 16 : d - 4) : d) * 16, v);
      if (d < 2) { const unsigned e = d ? 0u : 0x3f803f80u; st16(c.rs, t + 320 + d * 16, (u32x4){e, e, e, e}); }
    }
  }
}

__device__ __forceinline__ void retile_job(const Params& p, int j, const bf16*& W, unsigned& dst, int& cpr) {
  if (j < 160) { const int ks = j >> 3, blk = j & 7; W = p.wo + ks * 32 * D + blk * 80; dst = WS_WO + (blk * 20 + ks) * 5120; cpr = 10; }
  else if (j < 480) { const int e = j - 160, ks = e >> 4, blk = e & 15; W = p.w1 + ks * 32 * FF + blk * 96; dst = WS_W1 + (blk * 20 + ks) * 6144; cpr = 12; }
  else { const int e = j - 480, ks = e >> 3, blk = e & 7; W = p.w2 + ks * 32 * D + blk * 80; dst = WS_W2 + (blk * 48 + ks) * 5120; cpr = 10; }
}
__device__ __forceinline__ void retile_load(Ctx& c, const Params& p, int j, LAS char* base) {
  const bf16* W; unsigned dst; int cpr;
  retile_job(p, j, W, dst, cpr);
  const int ld = cpr == 12 ? FF : D;
#pragma unroll
  for (int i = 0; i < 6; i++) {
    const int q = i * 64 + c.lane, row = cpr == 12 ? q / 12 : q / 10, ch = q - row * cpr;
    if (i < 5 || cpr == 12) glds16<0>(W + row * ld + ch * 8, base + i * 1024);
  }
}
__device__ __forceinline__ void retile_store(Ctx& c, const Params& p, int j, LAS char* base) {
  const bf16* W; unsigned dst; int cpr;
  retile_job(p, j, W, dst, cpr);
  const int st = cpr * 16, pp = c.lane & 3;
  const unsigned ad = (unsigned)(size_t)base + (c.g * 8 + ((c.lane >> 2) & 3)) * st;
#pragma unroll
  for (int n = 0; n < 6; n++) {
    if (n < 5 || cpr == 12) {
      const int cq = (n >= 4 && cpr == 10) ? 16 + pp : 8 * (n >> 1) + 2 * pp + (n & 1);
      u32x2 r0, r1;
      asm volatile("ds_read_b64_tr_b16 %0, %2\n\tds_read_b64_tr_b16 %1, %3\n\ts_waitcnt lgkmcnt(0)"
                   : "=&v"(r0), "=&v"(r1) : "v"(ad + cq * 8), "v"(ad + cq * 8 + 4 * st) : "memory");
      st16(c.rs, dst + n * 1024 + c.lane * 16, (u32x4){r0[0], r0[1], r1[0], r1[1]});
    }
  }
}

constexpr int L_WO = 61440, L_W1 = 20480, L_CS = 143360;
__device__ __forceinline__ void ld16s(u32x4& v, const char* p) { asm volatile("global_load_dwordx4 %0, %1, off sc1" : "=v"(v) : "v"(p)); }
__device__ __forceinline__ void ld_frag(u32x4& v, const Ctx& c, unsigned soff) {
  asm volatile("buffer_load_dwordx4 %0, %1, %2, %3 offen sc1" : "=v"(v) : "v"(c.lane * 16), "s"(c.rd), "s"(soff));
}
__device__ __forceinline__ void ld16(u32x4& v, const void* p) { asm volatile("global_load_dwordx4 %0, %1, off" : "=v"(v) : "v"(p)); }
__device__ __forceinline__ void ld8(u32x2& v, const void* p) { asm volatile("global_load_dwordx2 %0, %1, off" : "=v"(v) : "v"(p)); }
template <int N> __device__ __forceinline__ void wait_reg(u32x4& v) { asm volatile("s_waitcnt vmcnt(%1)" : "+v"(v) : "n"(N)); }
__device__ __forceinline__ void raise(Ctx& c, unsigned flag) {
  if (c.tid == 0) __hip_atomic_store((u64*)(c.ws + WS_FLAG) + flag, c.token, __ATOMIC_RELAXED, __HIP_MEMORY_SCOPE_AGENT);
}
template <int J, int NT, int STR> __device__ __forceinline__ void rd_b(u32x4& v, const unsigned (&lb)[3]) {
  constexpr int t = J / NT, tt = STR == 10240 ? (t & 15) : t, per = 61440 / STR;
  asm volatile("ds_read_b128 %0, %1 offset:%2" : "=v"(v) : "v"(lb[tt / per]), "n"((tt % per) * STR + (J % NT) * 1024));
}
template <int OFF> __device__ __forceinline__ void wr_b(unsigned ad, const u32x4& v) {
  asm volatile("ds_write_b128 %0, %1 offset:%2" :: "v"(ad), "v"(v), "n"(OFF) : "memory");
}
template <int N> __device__ __forceinline__ void wait_lds(u32x4& v) { asm volatile("s_waitcnt lgkmcnt(%1)" : "+v"(v) : "n"(N)); }
template <int N> __device__ __forceinline__ void wait_lds(u32x2& v) { asm volatile("s_waitcnt lgkmcnt(%1)" : "+v"(v) : "n"(N)); }
template <int T0, int T1, int NT, int STR, bool FIRST, int JEND, int NA, int R, class WT>
__device__ __forceinline__ void k_steps(f32x4 (&acc)[NT], u32x4 (&a)[NA], u32x4 (&bf)[R], const unsigned (&lb)[3], WT wt) {
  if constexpr (FIRST) static_for<T0 * NT, T0 * NT + R - 1>([&](auto jc) { rd_b<jc.value, NT, STR>(bf[jc.value % R], lb); });
  static_for<T0 * NT, T1 * NT>([&](auto jc) {
    constexpr int j = jc.value, t = j / NT, n = j % NT;
    if constexpr (n == 0) wt(std::integral_constant<int, t>{});
    if constexpr (j + R - 1 < JEND) rd_b<j + R - 1, NT, STR>(bf[(j + R - 1) % R], lb);
    wait_lds<(j + R - 1 < JEND ? R - 1 : JEND - 1 - j)>(bf[j % R]);
    acc[n] = mma16(bf[j % R], a[t], acc[n]);
    SB();
  });
#pragma unroll
  for (int n = 0; n < NT; n++) asm volatile("" : "+v"(acc[n]));
}
template <bool WO>
__device__ __forceinline__ void epi80(Ctx& c, const Params& p, f32x4 (&acc)[5], const u32x4 (&e4)[2], const u32x2& e2, u32x2 (&hres)[5], int rb, int cb) {
  const int wm = c.w & 3, g = c.g, r16 = c.r16;
  LAS f32x4* red = (LAS f32x4*)c.lds + wm * 320 + c.lane;
  bar();
  if (c.w >= 4) {
#pragma unroll
    for (int n = 0; n < 5; n++) red[n * 64] = acc[n];
  }
  bar();
  if (c.w < 4) {
    const u32x2 ev[5] = {{e4[0][0], e4[0][1]}, {e4[0][2], e4[0][3]}, {e4[1][0], e4[1][1]}, {e4[1][2], e4[1][3]}, e2};
    u32x2 o[5];
#pragma unroll
    for (int n = 0; n < 5; n++) {
      f32x4 v = acc[n] + red[n * 64];
      v[0] += bflo(ev[n][0]); v[1] += bfhi(ev[n][0]); v[2] += bflo(ev[n][1]); v[3] += bfhi(ev[n][1]);
      if (!WO) { v[0] += bflo(hres[n][0]); v[1] += bfhi(hres[n][0]); v[2] += bflo(hres[n][1]); v[3] += bfhi(hres[n][1]); }
      o[n] = (u32x2){pk2(v[0], v[1]), pk2(v[2], v[3])};
    }
    const int row = rb * 64 + wm * 16 + r16;
    if (WO) {
      const int row16 = row >> 4;
#pragma unroll
      for (int q = 0; q < 2; q++) {
        const int c8 = cb * 10 + q * 4 + g;
        st16(c.rs, WS_H + ((row16 * 20 + (c8 >> 2)) * 64 + (c8 & 3) * 16 + r16) * 16, (u32x4){o[2 * q][0], o[2 * q][1], o[2 * q + 1][0], o[2 * q + 1][1]});
      }
      const int c8 = cb * 10 + 8 + (g >> 1);
      const auto x0 = __builtin_amdgcn_permlane16_swap(o[4][0], o[4][0], false, false);
      const auto x1 = __builtin_amdgcn_permlane16_swap(o[4][1], o[4][1], false, false);
      if (!(g & 1)) st16(c.rs, WS_H + ((row16 * 20 + (c8 >> 2)) * 64 + (c8 & 3) * 16 + r16) * 16, (u32x4){x0[0], x1[0], x0[1], x1[1]});
      float s1 = 0.f, s2 = 0.f;
#pragma unroll
      for (int n = 0; n < 5; n++) {
        hres[n] = o[n];
        const float a0 = bflo(o[n][0]), a1 = bfhi(o[n][0]), a2 = bflo(o[n][1]), a3 = bfhi(o[n][1]);
        s1 += (a0 + a1) + (a2 + a3);
        s2 += (a0 * a0 + a1 * a1) + (a2 * a2 + a3 * a3);
      }
      s1 += __shfl_xor(s1, 16); s2 += __shfl_xor(s2, 16);
      s1 += __shfl_xor(s1, 32); s2 += __shfl_xor(s2, 32);
      if (g == 0) st8(c.rs, WS_SP + row16 * 1024 + cb * 128 + r16 * 8, (u32x2){__builtin_bit_cast(unsigned, s1), __builtin_bit_cast(unsigned, s2)});
    } else {
      bf16* op = p.out + (size_t)row * D + cb * 80;
      *(u32x4*)(op + g * 8) = (u32x4){o[0][0], o[0][1], o[1][0], o[1][1]};
      *(u32x4*)(op + 32 + g * 8) = (u32x4){o[2][0], o[2][1], o[3][0], o[3][1]};
      *(u32x2*)(op + 64 + g * 4) = o[4];
    }
  }
}

__device__ __forceinline__ void tail_phases(Ctx& c, const Params& p, int xcd, int i, unsigned att_flag, u32x4 (&stash)[20]) {
  LAS char* lds = c.lds;
  const int lane = c.lane, w = c.w, r16 = c.r16, g = c.g;
  const int wm = w & 3, wk = w >> 2;
  const int rb64 = xcd * 4 + (i & 3), cb80 = i >> 2, rb128 = xcd * 2 + (i & 1), cb96 = i >> 1;
  const unsigned w1g = WS_W1 + cb96 * 122880, w2g = WS_W2 + cb80 * 245760, l16 = lane * 16;
  auto w2_fetch = [&](int slot) {
    const int ts = slot / 10, r = slot - ts * 10, h = r >= 5;
    blds16(c.rs, l16, w2g + ((h * 24 + (ts)) * 5 + r - 5 * h) * 1024, lds + slot * 1024);
  };
  const f32x4 z4 = {0.f, 0.f, 0.f, 0.f};
  u32x4 e4[2];
  u32x2 e2, hres[5];
  rawbar();
  if (w < 6) {
#pragma unroll
    for (int q = 0; q < 6; q++) blds16(c.rs, l16, w1g + ((w) * 6 + q) * 1024, lds + L_W1 + (w * 6 + q) * 1024);
  }
  {
    const bf16* xr = p.x + (size_t)(rb64 * 64 + wm * 16 + r16) * D + cb80 * 80;
    ld16(e4[0], xr + g * 8); ld16(e4[1], xr + 32 + g * 8); ld8(e2, xr + 64 + g * 4);
  }
  if (w < 6) wait_vm<9>(); else wait_vm<3>();
  rawbar();
  raise(c, att_flag);
  STAMP(7);
  await(c, F_ATT + xcd * 32 + lane, 32, 0, 0);
  STAMP(8);
  {
    u32x4 a[10], bf[11];
    const unsigned ag = WS_O + ((rb64 * 4 + wm) * 20 + wk * 10) * 1024;
#pragma unroll
    for (int t = 0; t < 10; t++) ld_frag(a[t], c, ag + (t) * 1024);
    const unsigned l0 = (unsigned)(size_t)lds + L_WO + wk * 51200 + lane * 16, lb[3] = {l0, l0, l0};
    f32x4 acc[5] = {z4, z4, z4, z4, z4};
    k_steps<0, 10, 5, 5120, true, 50>(acc, a, bf, lb, [&](auto tc) { wait_reg<9 - tc.value>(a[tc.value]); });
    STAMP(9);
    asm volatile("" : "+v"(e4[0]), "+v"(e4[1]), "+v"(e2));
    epi80<true>(c, p, acc, e4, e2, hres, rb64, cb80);
  }
  rawbar();
  if (w < 4) wait_vm<0>();
  else if (w < 7) {
#pragma unroll 1
    for (int t = 2 + w; t < 20; t += 3) {
#pragma unroll
      for (int q = 0; q < 6; q++) blds16(c.rs, l16, w1g + ((t) * 6 + q) * 1024, lds + L_W1 + (t * 6 + q) * 1024);
    }
  }
  rawbar();
  raise(c, F_WO + rb64 * 8 + cb80);
  STAMP(10);
  wait_vm<0>();
  await(c, F_WO + rb128 * 16 + lane, 16, 0, 0);
  STAMP(11);
  {
    u32x4 a[20], bf[12], bv[3];
    u32x2 sp0, sp1;
    const int row16 = rb128 * 8 + w;
    {
      const char* q = c.ws + WS_SP + row16 * 1024 + g * 256 + r16 * 8;
      asm volatile("global_load_dwordx2 %0, %2, off sc1\n\tglobal_load_dwordx2 %1, %2, off offset:128 sc1" : "=&v"(sp0), "=&v"(sp1) : "v"(q));
    }
    const unsigned ag = WS_H + row16 * 20480;
#pragma unroll
    for (int t = 0; t < 12; t++) ld_frag(a[t], c, ag + (t) * 1024);
#pragma unroll
    for (int j = 0; j < 3; j++) ld16(bv[j], p.b1 + cb96 * 96 + j * 32 + g * 8);
#pragma unroll
    for (int q = 0; q < 4; q++) { const int s = w * 4 + q; w2_fetch(s < 20 ? s : (s < 30 ? s + 130 : 159)); }
    LAS char* bl = lds + L_W1 + lane * 16;
    LAS float* st_cs = (LAS float*)(lds + L_CS);
    if (w < 6) {
      const u32x4 ones = {0x3f803f80u, 0x3f803f80u, 0x3f803f80u, 0x3f803f80u};
      f32x4 cs0 = z4, cs1 = z4;
#pragma unroll
      for (int t = 0; t < 20; t += 2) {
        cs0 = mma16(ones, *(LAS u32x4*)(bl + (t * 6 + w) * 1024), cs0);
        cs1 = mma16(ones, *(LAS u32x4*)(bl + (t * 6 + 6 + w) * 1024), cs1);
      }
      if (g == 0) st_cs[w * 16 + r16] = cs0[0] + cs1[0];
    }
    f32x4 acc[6] = {z4, z4, z4, z4, z4, z4};
    const unsigned l0 = (unsigned)(size_t)bl, lb[3] = {l0, l0 + 61440, l0};
    asm volatile("s_waitcnt lgkmcnt(0)" ::: "memory");
    k_steps<0, 20, 6, 6144, true, 120>(acc, a, bf, lb, [&](auto tc) {
      constexpr int t = tc.value;
      if constexpr (t >= 1 && t <= 8) ld_frag(a[t + 11], c, ag + (t + 11) * 1024);
      wait_reg<(t < 12 ? 18 - t + (t < 8 ? t : 8) : 19 - t)>(a[t]);
    });
    STAMP(12);
    asm volatile("" : "+v"(sp0), "+v"(sp1), "+v"(bv[0]), "+v"(bv[1]), "+v"(bv[2]));
    const f32x2 q2 = __builtin_bit_cast(f32x2, sp0) + __builtin_bit_cast(f32x2, sp1);
    float s1 = q2[0], s2 = q2[1];
    s1 += __shfl_xor(s1, 16); s2 += __shfl_xor(s2, 16);
    s1 += __shfl_xor(s1, 32); s2 += __shfl_xor(s2, 32);
    const float mean = s1 * (1.f / D);
    const float rs = __builtin_amdgcn_rsqf(fmaxf(s2 * (1.f / D) - mean * mean, 0.f) + 1e-5f);
    bar();
#pragma unroll
    for (int j = 0; j < 3; j++) {
      u32x4 o;
#pragma unroll
      for (int h = 0; h < 2; h++) {
        const int n = 2 * j + h;
        const f32x4 c4 = *(LAS f32x4*)(st_cs + n * 16 + g * 4);
        const float v0 = fmaxf(rs * (acc[n][0] - mean * c4[0]) + bflo(bv[j][2 * h]), 0.f);
        const float v1 = fmaxf(rs * (acc[n][1] - mean * c4[1]) + bfhi(bv[j][2 * h]), 0.f);
        const float v2 = fmaxf(rs * (acc[n][2] - mean * c4[2]) + bflo(bv[j][2 * h + 1]), 0.f);
        const float v3 = fmaxf(rs * (acc[n][3] - mean * c4[3]) + bfhi(bv[j][2 * h + 1]), 0.f);
        o[2 * h] = pk2(v0 * v0, v1 * v1); o[2 * h + 1] = pk2(v2 * v2, v3 * v3);
      }
      st16(c.rs, WS_G + (row16 * 48 + cb96 * 3 + j) * 1024 + lane * 16, o);
    }
  }
  bar();
  if (w < 5) {
#pragma unroll
    for (int q = 0; q < 10; q++) w2_fetch(20 + w * 10 + q);
  }
  static_for<0, 10>([&](auto qc) { wr_b<qc.value * 1024>((unsigned)(size_t)lds + (70 + w * 10) * 1024 + lane * 16, stash[qc.value]); });
  {
    const bf16* br = p.b2 + cb80 * 80;
    ld16(e4[0], br + g * 8); ld16(e4[1], br + 32 + g * 8); ld8(e2, br + 64 + g * 4);
  }
  if (w < 5) wait_vm<13>(); else wait_vm<3>();
  bar();
  raise(c, F_FF1 + rb128 * 16 + cb96);
  STAMP(13);
  wait_vm<0>();
  await(c, F_FF1 + (rb64 >> 1) * 16 + lane, 16, 0, 0);
  STAMP(14);
  {
    u32x4 a[24], bf[11];
    const unsigned ag = WS_G + ((rb64 * 4 + wm) * 48 + wk * 24) * 1024;
#pragma unroll
    for (int t = 0; t < 24; t++) ld_frag(a[t], c, ag + (t) * 1024);
    const unsigned l0 = (unsigned)(size_t)lds + wk * 5120 + lane * 16, lb[3] = {l0, l0 + 61440, l0 + 122880};
    f32x4 acc[5] = {z4, z4, z4, z4, z4};
    k_steps<0, 8, 5, 10240, true, 80>(acc, a, bf, lb, [&](auto tc) { wait_reg<23 - tc.value>(a[tc.value]); });
    rawbar();
    asm volatile("s_waitcnt lgkmcnt(0)" ::: "memory");
    static_for<0, 10>([&](auto qc) { wr_b<qc.value * 1024>((unsigned)(size_t)lds + w * 10240 + lane * 16, stash[10 + qc.value]); });
    k_steps<8, 16, 5, 10240, false, 80>(acc, a, bf, lb, [&](auto tc) { wait_reg<23 - tc.value>(a[tc.value]); });
    rawbar();
    k_steps<16, 24, 5, 10240, true, 120>(acc, a, bf, lb, [&](auto tc) { wait_reg<23 - tc.value>(a[tc.value]); });
    STAMP(15);
    asm volatile("" : "+v"(e4[0]), "+v"(e4[1]), "+v"(e2));
    epi80<false>(c, p, acc, e4, e2, hres, rb64, cb80);
  }
}

__device__ __forceinline__ f32x16 mma32(u32x4 a, u32x4 b, f32x16 c) {
  return __builtin_amdgcn_mfma_f32_32x32x16_bf16(__builtin_bit_cast(bf16x8, a), __builtin_bit_cast(bf16x8, b), c, 0, 0, 0);
}
__device__ __forceinline__ f32x16 mma32h(u32x2 a, u32x2 b, f32x16 c) {
  return __builtin_amdgcn_mfma_f32_32x32x8bf16_1k(__builtin_bit_cast(s16x4, a), __builtin_bit_cast(s16x4, b), c, 0, 0, 0);
}
__device__ __forceinline__ float max16(const f32x16& s) {
  float m = fmaxf(fmaxf(s[0], s[1]), s[2]);
#pragma unroll
  for (int i = 3; i < 15; i += 2) m = fmaxf(fmaxf(m, s[i]), s[i + 1]);
  return fmaxf(m, s[15]);
}
__device__ __forceinline__ u32x2 set_shift(u32x2 qhi, float m, int hh) {
  const float a = bflo(pk2(m, 0.f));
  const float r = m - a;
  const float b = bflo(pk2(r, 0.f));
  const u32x2 e = {pk2(-a, -b), pk2(b - r, 0.f)};
  return hh ? e : qhi;
}
__device__ __forceinline__ u32x2 exp4(const f32x16& s, int i) {
  const float e0 = __builtin_amdgcn_exp2f(s[i]), e1 = __builtin_amdgcn_exp2f(s[i + 1]);
  const float e2 = __builtin_amdgcn_exp2f(s[i + 2]), e3 = __builtin_amdgcn_exp2f(s[i + 3]);
  return (u32x2){pk2(e0, e1), pk2(e2, e3)};
}

template <int OFF> __device__ __forceinline__ void rd8(u32x2& v, unsigned ad) { asm volatile("ds_read_b64 %0, %1 offset:%2" : "=v"(v) : "v"(ad), "n"(OFF)); }
template <int OFF> __device__ __forceinline__ void rd16(u32x4& v, unsigned ad) { asm volatile("ds_read_b128 %0, %1 offset:%2" : "=v"(v) : "v"(ad), "n"(OFF)); }
__device__ __forceinline__ unsigned local_s(unsigned v) { asm volatile("" : "+s"(v)); return v; }
__device__ __forceinline__ void wait_k(u32x2& a, u32x2& b, u32x2& h, u32x4& v) {
  asm volatile("s_waitcnt lgkmcnt(2)" : "+v"(a), "+v"(b), "+v"(h), "+v"(v));
}
template <class T> __device__ __forceinline__ void pin(T& v) { asm volatile("" : "+v"(v)); }
__device__ __forceinline__ void ld_flags(u32x4& a, u32x4& b, const char* fp) {
  asm volatile("global_load_dwordx4 %0, %2, off sc1\n\tglobal_load_dwordx4 %1, %2, off offset:16 sc1" : "=&v"(a), "=&v"(b) : "v"(fp));
}

constexpr int SCHED[] = {300, 0, 1, 106, 107, 2, 3, 301, 4, 5, 100, 101, 6, 7, 302, 8, 9, 102, 103, 311, 10, 11, 303, 12, 13, 104, 105, 310, 14, 15};

__device__ __forceinline__ void attn_phase(Ctx& c, const Params& p, int head, int qb, int i, u32x4 (&stash)[20]) {
  LAS char* lds = c.lds;
  const int lane = c.lane, w = c.w;
  const int r = lane & 31, hh = lane >> 5;
  const int qrow = qb * 256 + w * 32 + r;
  const unsigned kvg = WS_KV + head * KV_HEAD;
  const char* qp = c.ws + WS_Q + (size_t)(head * 5 * S + qrow) * 8;
  u32x2 qa, qc, qhi0;
  asm volatile("global_load_dwordx2 %0, %1, off sc1" : "=v"(qa) : "v"(qp + (size_t)(2 * hh) * S * 8));
  asm volatile("global_load_dwordx2 %0, %1, off sc1" : "=v"(qc) : "v"(qp + (size_t)(2 * hh + 1) * S * 8));
  asm volatile("global_load_dwordx2 %0, %1, off sc1" : "=v"(qhi0) : "v"(qp + (size_t)4 * S * 8));
  auto issue1 = [&](int s, int j) {
    if (j < 5 || w < 6) blds16(c.rs, lane * 16, kvg + s * AT_STAGE + (w + 8 * j) * 1024, lds + (s % 3) * AT_STAGE + (w + 8 * j) * 1024);
  };
  auto issue = [&](int s) {
#pragma unroll
    for (int j = 0; j < 6; j++) issue1(s, j);
  };
  const int job = blockIdx.x * 3 + w;
  LAS char* jl = lds + 3 * AT_STAGE + w * 6144;
  issue(0);
  if (w < 3) retile_load(c, p, job, jl);
  issue(1); issue(2);
  if (w < 3) wait_vm<17>(); else if (w < 6) wait_vm<12>(); else wait_vm<10>();
  asm volatile("" : "+v"(qa), "+v"(qc), "+v"(qhi0));
  const u32x4 qlo = {qa[0], qa[1], qc[0], qc[1]};
  bar();
  STAMP(5);

  const int koff = hh * 512 + r * 8, khoff = 1024 + lane * 8, voff = 1536 + hh * 352 + (r <= 20 ? r : 21) * 16;
  const u32x2 qz = hh ? (u32x2){0u, 0u} : qhi0;
  const f32x16 zero = {0.f, 0.f, 0.f, 0.f, 0.f, 0.f, 0.f, 0.f, 0.f, 0.f, 0.f, 0.f, 0.f, 0.f, 0.f, 0.f};
  u32x2 qhi;
  f32x16 sc;
  {
    const u32x2 k0a = *(LAS u32x2*)(lds + koff), k0b = *(LAS u32x2*)(lds + koff + 256);
    const u32x4 k0 = {k0a[0], k0a[1], k0b[0], k0b[1]};
    const u32x2 kh0 = *(LAS u32x2*)(lds + khoff);
    const f32x16 s0 = mma32(k0, qlo, zero);
    float m = max16(mma32h(kh0, qz, s0));
    m = fmaxf(m, __shfl_xor(m, 32));
    qhi = set_shift(qhi0, m, hh);
    sc = mma32h(kh0, qhi, s0);
  }
  f32x16 o = zero;
  const unsigned l0 = (unsigned)(size_t)lds;
  u32x2 ka, kb, khi;
  u32x4 v0, v1;
  rd8<KV_TILE>(ka, l0 + koff); rd8<KV_TILE + 256>(kb, l0 + koff); rd8<KV_TILE>(khi, l0 + khoff);
  rd16<0>(v0, l0 + voff); rd16<704>(v1, l0 + voff);
  u32x4 p1p = {0u, 0u, 0u, 0u}, v1p = p1p, fr0 = p1p, fr1 = p1p;
  const int cb80 = i >> 2;
  unsigned ak[2], ah[2], av[2];
  float el[4] = {0.f, 0.f, 0.f, 0.f};
  auto slot = [&](auto tc, auto lc) {
    constexpr int t = tc.value;
    constexpr bool LAST = lc.value;
    constexpr int xk = t + 2 >= 16, ok = ((t + 2) & 15) * KV_TILE, xv = t + 1 >= 16, ov = ((t + 1) & 15) * KV_TILE;
    if constexpr (t == 14 && !LAST) __builtin_amdgcn_s_setprio(3);
    if constexpr (t == 4) __builtin_amdgcn_s_setprio(2);
    if constexpr (t == 9) __builtin_amdgcn_s_setprio(1);
    if constexpr (t == 12) __builtin_amdgcn_s_setprio(0);
    f32x16 sn;
    float e[16];
    unsigned pp[6];
    static_for<0, sizeof(SCHED) / sizeof(int)>([&](auto oc) {
      constexpr int op = SCHED[oc.value];
      if constexpr (op < 16) { e[op] = __builtin_amdgcn_exp2f(sc[op]); if constexpr (op >= 12) pin(e[op]); }
      else if constexpr (op < 106) { pp[op - 100] = pk2(e[2 * (op - 100)], e[2 * (op - 100) + 1]); pin(pp[op - 100]); }
      else if constexpr (op < 108) p1p[op - 104] = pk2(el[2 * (op - 106)], el[2 * (op - 106) + 1]);
      else if constexpr (op == 300) {
        sn = mma32((u32x4){ka[0], ka[1], kb[0], kb[1]}, qlo, zero);
      } else if constexpr (op == 301) {
        sn = mma32h(khi, qhi, sn);
        SB();
        rd8<ok>(ka, ak[xk]); rd8<ok + 256>(kb, ak[xk]); rd8<ok>(khi, ah[xk]);
      } else if constexpr (op == 302) {
        o = mma32(v1p, p1p, o);
      } else if constexpr (op == 310) {
        wait_k(ka, kb, khi, v1p);
      } else if constexpr (op == 311) {
        wait_lds<4>(v0);
      } else {
        o = mma32(v0, (u32x4){pp[0], pp[1], pp[2], pp[3]}, o);
        SB();
        v1p = v1;
        rd16<ov>(v0, av[xv]); rd16<ov + 704>(v1, av[xv]);
      }
      SB();
    });
    p1p[0] = pp[4]; p1p[1] = pp[5];
    el[0] = e[12]; el[1] = e[13]; el[2] = e[14]; el[3] = e[15];
    sc = sn;
  };
  auto warm = [&](unsigned voff, unsigned soff) {
    __builtin_amdgcn_raw_ptr_buffer_load_lds(c.rs, (LAS void*)(jl + 6144 * (3 - w)), 4, voff, soff, 0, 16);
  };
  const unsigned w1h = WS_W1 + (i >> 1) * 122880 + (i & 1) * 61440, w2p = WS_W2 + cb80 * 245760;
  const unsigned w2w = w2p + w * 5120;
  const unsigned w1v = w * 8192 + lane * 128 < 61440 ? w * 8192 + lane * 128 : 0;
  __builtin_amdgcn_s_setprio(3);
  wait_k(ka, kb, khi, v1p);
#pragma unroll 1
  for (int s = 0; s < 2; s++) {
    const unsigned cb = l0 + s * AT_STAGE, nb = cb + AT_STAGE;
    ak[0] = cb + koff; ak[1] = nb + koff; ah[0] = cb + khoff; ah[1] = nb + khoff; av[0] = cb + voff; av[1] = nb + voff;
    static_for<0, 16>([&](auto tc) {
      constexpr int t = tc.value;
      if constexpr (t == 4) { if (s == 0 && w < 3) { wait_vm<12>(); retile_store(c, p, local_s(job), jl); } }
      if constexpr (t == 9) { if (s == 1 && w == 7) ld_flags(fr0, fr1, c.ws + WS_FLAG + (F_RT + lane * 4) * 8); }
      if constexpr (t == 14) {
        CYC3(23);
        if (s == 0 && w >= 3) { if (w < 6) wait_vm<6>(); else wait_vm<5>(); }
        else wait_vm<0>();
        if (s == 1 && w == 7) {
          wait_reg<0>(fr0); wait_reg<0>(fr1);
          const unsigned lo = (unsigned)c.token, hi = (unsigned)(c.token >> 32);
          if (!__all(fr0[0] == lo && fr0[1] == hi && fr0[2] == lo && fr0[3] == hi && fr1[0] == lo && fr1[1] == hi && fr1[2] == lo && fr1[3] == hi)) {
            spin_flags(c, F_RT + lane, 64, F_RT + 64 + lane, 64);
            spin_flags(c, F_RT + 128 + lane, 64, F_RT + 192 + lane, 64);
          }
        }
        rawbar();
        CYC3(24);
        if (s == 0) raise(c, F_RT + blockIdx.x);
      }
      slot(tc, std::false_type{});
    });
  }
  ak[0] = ak[1]; ah[0] = ah[1]; av[0] = av[1];
  ak[1] = l0 + koff; ah[1] = l0 + khoff; av[1] = l0 + voff;
  static_for<0, 16>([&](auto tc) {
    constexpr int t = tc.value;
    if constexpr (t < 6) issue1(3, t);
    else {
      constexpr int q0 = 2 * (t - 6), q1 = q0 + 1;
      ld_frag(stash[q0], c, w2w + (((q0 / 5 & 1) * 24 + (q0 < 10 ? 7 : 16)) * 5 + q0 % 5) * 1024);
      ld_frag(stash[q1], c, w2w + (((q1 / 5 & 1) * 24 + (q1 < 10 ? 7 : 16)) * 5 + q1 % 5) * 1024);
      if constexpr (t % 2 == 1 && t < 14) { if (w == (t - 7) / 2) warm(w1v, w1h); }
    }
    if constexpr (t == 14) { CYC(27); wait_vm<18>(); rawbar(); CYC(28); }
    SB();
    slot(tc, std::false_type{});
  });
  ak[0] = ak[1]; ah[0] = ah[1]; av[0] = av[1];
  static_for<0, 16>([&](auto tc) {
    constexpr int t = tc.value;
    if constexpr (t == 2) { CYC(29); wait_vm<0>(); rawbar(); CYC(30); }
    if constexpr (t >= 2 && t < 15) {
      if (t < 14 || w < 4) blds16(c.rs, lane * 16, WS_WO + cb80 * 102400 + (w + 8 * (t - 2)) * 1024, lds + L_WO + (w + 8 * (t - 2)) * 1024);
      if constexpr (t % 3 == 0) { if (w == 3 + t / 3) __builtin_amdgcn_raw_ptr_buffer_load_lds(c.rs, (LAS void*)(lds + AT_STAGE + w * 256), 4, w1v, w1h, 0, 16); }
      if constexpr (t % 3 == 1 && t < 13) { if (w == t / 3 - 1) __builtin_amdgcn_raw_ptr_buffer_load_lds(c.rs, (LAS void*)(lds + AT_STAGE + w * 256), 4, (w * 64 + lane < 140 ? w * 64 + lane : 0) * 128, w2p + (i & 1) * 122880 + ((i >> 1) & 1) * 17920, 0, 16); }
    }
    SB();
    slot(tc, std::true_type{});
  });
  __builtin_amdgcn_s_setprio(0);
  wait_lds<0>(v1p); wait_lds<0>(ka); wait_lds<0>(kb); wait_lds<0>(khi); wait_lds<0>(v0); wait_lds<0>(v1);
  p1p[2] = pk2(el[0], el[1]); p1p[3] = pk2(el[2], el[3]);
  o = mma32(v1p, p1p, o);
  STAMP(6);
  float l = __shfl(o[8], r + 32);
  if (!__all(l < 1.8446744e19f)) {
    const unsigned kb = WS_KV + head * KV_HEAD;
    auto gk = [&](int t) {
      const u32x2 a = __builtin_amdgcn_raw_buffer_load_b64(c.rs, kb + t * KV_TILE + koff, 0, 16);
      const u32x2 b = __builtin_amdgcn_raw_buffer_load_b64(c.rs, kb + t * KV_TILE + koff + 256, 0, 16);
      return mma32((u32x4){a[0], a[1], b[0], b[1]}, qlo, zero);
    };
    float m = -__builtin_inff();
#pragma unroll 1
    for (int t = 0; t < 64; t++)
      m = fmaxf(m, max16(mma32h(__builtin_amdgcn_raw_buffer_load_b64(c.rs, kb + t * KV_TILE + khoff, 0, 16), qz, gk(t))));
    m = fmaxf(m, __shfl_xor(m, 32));
    qhi = set_shift(qhi0, m, hh);
    o = zero;
#pragma unroll 1
    for (int t = 0; t < 64; t++) {
      const f32x16 s2 = mma32h(__builtin_amdgcn_raw_buffer_load_b64(c.rs, kb + t * KV_TILE + khoff, 0, 16), qhi, gk(t));
      const u32x2 pa = exp4(s2, 0), pb = exp4(s2, 4), pc = exp4(s2, 8), pd = exp4(s2, 12);
      o = mma32(__builtin_amdgcn_raw_buffer_load_b128(c.rs, kb + t * KV_TILE + voff, 0, 16), (u32x4){pa[0], pa[1], pb[0], pb[1]}, o);
      o = mma32(__builtin_amdgcn_raw_buffer_load_b128(c.rs, kb + t * KV_TILE + voff + 704, 0, 16), (u32x4){pc[0], pc[1], pd[0], pd[1]}, o);
    }
    l = __shfl(o[8], r + 32);
  }
  const float inv = 1.f / l;
  const auto x0 = __builtin_amdgcn_permlane32_swap(pk2(o[0] * inv, o[1] * inv), pk2(o[4] * inv, o[5] * inv), false, false);
  const auto x1 = __builtin_amdgcn_permlane32_swap(pk2(o[2] * inv, o[3] * inv), pk2(o[6] * inv, o[7] * inv), false, false);
  const int ca = head * DH + (head & 1) * 4 + hh * 8, cr = head * DH + ((head & 1) ? 0 : 16);
  const unsigned ob = WS_O + (qrow >> 4) * 20480 + (qrow & 15) * 16;
  st16(c.rs, ob + (ca >> 3) * 256, (u32x4){x0[0], x1[0], x0[1], x1[1]});
  if (hh == 0) st8(c.rs, ob + (cr >> 3) * 256 + (cr & 4) * 2, (u32x2){pk2(o[8] * inv, o[9] * inv), pk2(o[10] * inv, o[11] * inv)});
}

__global__ __launch_bounds__(512, 2) void k_block(Params p) {
  extern __shared__ __attribute__((aligned(16))) char smem[];
  Ctx c;
  c.tid = threadIdx.x; c.lane = c.tid & 63; c.w = __builtin_amdgcn_readfirstlane(c.tid >> 6);
  c.r16 = c.lane & 15; c.g = c.lane >> 4;
  c.lds = (LAS char*)smem; c.ws = p.ws;
  c.rs = __builtin_amdgcn_make_buffer_rsrc(p.ws, 0, WS_END, 0x00020000);
  c.rd = (u32x4){(unsigned)(size_t)p.ws, (unsigned)((size_t)p.ws >> 32) & 0xffffu, WS_END, 0x00020000u};
  STAMP(0);
  const u64 epoch = __hip_atomic_load((const u64*)(p.ws + WS_FLAG) + F_EPOCH, __ATOMIC_RELAXED, __HIP_MEMORY_SCOPE_AGENT);
  const int b = blockIdx.x, xcd = b & 7, i = b >> 3;
  {
    const int rb = (xcd & 3) * 4 + (i & 3), cb = (xcd >> 2) * 8 + (i >> 2);
    if (cb < 10) qkv_phase<false>(c, p, rb, cb);
    else if (cb < 15) qkv_phase<true>(c, p, rb, cb);
    else if (c.w < 6) {
      retile_load(c, p, 768 + rb * 6 + c.w, c.lds + c.w * 6144);
      wait_vm<0>();
      retile_store(c, p, 768 + rb * 6 + c.w, c.lds + c.w * 6144);
    }
    u64 z = (epoch + 0x9e3779b97f4a7c15ull) * 0xbf58476d1ce4e5b9ull;
    c.token = z ^ (z >> 31);
    if (cb < 15) publish(c, F_QKV + cb * 16 + rb);
    STAMP(3);
  }
  const int head = xcd * 4 + (i & 3), qb = i >> 2;
  {
    const int c0 = head * 20 / 128, c1 = (head * 20 + 19) / 128, j = c.lane >> 4;
    await(c, F_QKV + (((j & 1) ? c1 : c0) + (j >> 1) * 5) * 16 + c.r16, 64, F_QKV + (((j & 1) ? c1 : c0) + 10) * 16 + c.r16, 32);
  }
  STAMP(4);
  u32x4 stash[20];
  attn_phase(c, p, head, qb, i, stash);
  tail_phases(c, p, xcd, i, F_ATT + qb * 32 + head, stash);
  if (b == 0 && c.tid == 0) __hip_atomic_store((u64*)(p.ws + WS_FLAG) + F_EPOCH, epoch + 1, __ATOMIC_RELAXED, __HIP_MEMORY_SCOPE_AGENT);
}

extern "C" void kernel_launch(void* const* d_in, const int* in_sizes, int n_in, void* d_out, int out_size, void* d_ws,
                              size_t ws_size, hipStream_t stream) {
  Params p;
  p.x = (const bf16*)d_in[0]; p.wqkv = (const bf16*)d_in[1]; p.wo = (const bf16*)d_in[2];
  p.w1 = (const bf16*)d_in[4]; p.b1 = (const bf16*)d_in[5]; p.w2 = (const bf16*)d_in[6]; p.b2 = (const bf16*)d_in[7];
  p.out = (bf16*)d_out; p.ws = (char*)d_ws;
  (void)hipFuncSetAttribute((const void*)k_block, hipFuncAttributeMaxDynamicSharedMemorySize, LDS_BYTES);
  k_block<<<256, 512, LDS_BYTES, stream>>>(p);
}
```

```cpp
#include <hip/hip_runtime.h>
#include <hip/hip_bf16.h>
#include <stdint.h>
#include <type_traits>

#define LAS __attribute__((address_space(3)))
#define GAS __attribute__((address_space(1)))
typedef __bf16 bf16;
typedef unsigned long long u64;
typedef __bf16 bf16x2 __attribute__((ext_vector_type(2)));
typedef __bf16 bf16x8 __attribute__((ext_vector_type(8)));
typedef short s16x4 __attribute__((ext_vector_type(4)));
typedef float f32x2 __attribute__((ext_vector_type(2)));
typedef float f32x4 __attribute__((ext_vector_type(4)));
typedef float f32x16 __attribute__((ext_vector_type(16)));
typedef unsigned u32x2 __attribute__((ext_vector_type(2)));
typedef unsigned u32x4 __attribute__((ext_vector_type(4)));
typedef __amdgpu_buffer_rsrc_t rsrc_t;

constexpr int S = 2048, D = 640, NH = 32, DH = 20, FF = 1536;
constexpr int KV_TILE = 3072;
constexpr int KV_HEAD = 64 * KV_TILE;
constexpr int AT_STAGE = 16 * KV_TILE;
constexpr int LDS_BYTES = 163840;
constexpr unsigned WS_FLAG = 0;
constexpr unsigned F_QKV = 0, F_ATT = 256, F_WO = 512, F_FF1 = 768, F_RT = 1024, F_EPOCH = 1536;
constexpr unsigned WS_Q = 16384;
constexpr unsigned WS_KV = WS_Q + NH * S * 40;
constexpr unsigned WS_O = WS_KV + NH * KV_HEAD;
constexpr unsigned WS_H = WS_O + S * D * 2;
constexpr unsigned WS_G = WS_H + S * D * 2;
constexpr unsigned WS_WO = WS_G + S * FF * 2;
constexpr unsigned WS_W1 = WS_WO + D * D * 2;
constexpr unsigned WS_W2 = WS_W1 + D * FF * 2;
constexpr unsigned WS_SP = WS_W2 + D * FF * 2;
constexpr unsigned WS_END = WS_SP + S * 64;

#define SB() __builtin_amdgcn_sched_barrier(0)
template <int N> __device__ __forceinline__ void wait_vm() { asm volatile("s_waitcnt vmcnt(%0)" ::"n"(N) : "memory"); }
__device__ __forceinline__ void bar() { asm volatile("s_waitcnt lgkmcnt(0)" ::: "memory"); __builtin_amdgcn_s_barrier(); asm volatile("" ::: "memory"); }
__device__ __forceinline__ void rawbar() { asm volatile("" ::: "memory"); __builtin_amdgcn_s_barrier(); asm volatile("" ::: "memory"); }
template <int AUX> __device__ __forceinline__ void glds16(const void* g, LAS char* l) {
  __builtin_amdgcn_global_load_lds((const GAS void*)g, (LAS void*)l, 16, 0, AUX);
}
__device__ __forceinline__ void blds16(rsrc_t r, unsigned voff, unsigned soff, LAS char* l) {
  __builtin_amdgcn_raw_ptr_buffer_load_lds(r, (LAS void*)l, 16, voff, soff, 0, 16);
}
__device__ __forceinline__ void st16(rsrc_t r, unsigned off, u32x4 v) { __builtin_amdgcn_raw_buffer_store_b128(v, r, off, 0, 16); }
__device__ __forceinline__ void st8(rsrc_t r, unsigned off, u32x2 v) { __builtin_amdgcn_raw_buffer_store_b64(v, r, off, 0, 16); }
__device__ __forceinline__ unsigned pk2(float a, float b) {
  f32x2 f = {a, b};
  return __builtin_bit_cast(unsigned, __builtin_convertvector(f, bf16x2));
}
__device__ __forceinline__ float bflo(unsigned u) { return __builtin_bit_cast(float, u << 16); }
__device__ __forceinline__ float bfhi(unsigned u) { return __builtin_bit_cast(float, u & 0xffff0000u); }
__device__ __forceinline__ f32x4 mma16(u32x4 a, u32x4 b, f32x4 c) {
  return __builtin_amdgcn_mfma_f32_16x16x32_bf16(__builtin_bit_cast(bf16x8, a), __builtin_bit_cast(bf16x8, b), c, 0, 0, 0);
}
template <int I, int N, class F> __device__ __forceinline__ void static_for(F&& f) {
  if constexpr (I < N) { f(std::integral_constant<int, I>{}); static_for<I + 1, N>(f); }
}

struct Params {
  const bf16 *x, *wqkv, *wo, *w1, *b1, *w2, *b2;
  bf16* out;
  char* ws;
};
struct Ctx {
  int tid, lane, w, r16, g;
  LAS char* lds;
  char* ws;
  rsrc_t rs;
  u32x4 rd;
  u64 token;
};
#define CYC(i) do {} while (0)
#define CYC3(i) do {} while (0)
#define STAMP(i) do {} while (0)

__device__ __forceinline__ void publish(Ctx& c, unsigned flag) {
  wait_vm<0>();
  bar();
  if (c.tid == 0) __hip_atomic_store((u64*)(c.ws + WS_FLAG) + flag, c.token, __ATOMIC_RELAXED, __HIP_MEMORY_SCOPE_AGENT);
}
__device__ __forceinline__ void spin_flags(Ctx& c, unsigned idx0, int n0, unsigned idx1, int n1) {
  const u64* f = (const u64*)(c.ws + WS_FLAG);
  for (int it = 0; it < (1 << 20); it++) {
    u64 a = c.token, b = c.token;
    if (c.lane < n0) a = __hip_atomic_load(f + idx0, __ATOMIC_RELAXED, __HIP_MEMORY_SCOPE_AGENT);
    if (c.lane < n1) b = __hip_atomic_load(f + idx1, __ATOMIC_RELAXED, __HIP_MEMORY_SCOPE_AGENT);
    if (__all(a == c.token && b == c.token)) break;
    __builtin_amdgcn_s_sleep(1);
  }
}
__device__ __forceinline__ void await(Ctx& c, unsigned idx0, int n0, unsigned idx1, int n1) {
  if (c.w == 7) spin_flags(c, idx0, n0, idx1, n1);
  rawbar();
}

template <bool ISV>
__device__ __forceinline__ void qkv_phase(Ctx& c, const Params& p, int rb, int cb) {
  constexpr int NS = 4, NKT = 10, STAGE = 32768, LDB = 3 * D;
  LAS char* lds = c.lds;
  LAS float* st_mean = (LAS float*)(lds + NS * STAGE);
  LAS float* st_rstd = st_mean + 128;
  LAS float* st_cs = st_rstd + 128;
  const int lane = c.lane, w = c.w, r16 = c.r16, g = c.g;
  const int wm = w & 3, wn = w >> 2;
  const int row0 = rb * 128, col0 = cb * 128;
  const int rot = (rb + cb) % NKT;

  const char* srcA[2];
  const char* srcB[2];
#pragma unroll
  for (int i = 0; i < 2; i++) {
    const int pc = w + 8 * i;
    {
      const int lr = pc * 8 + (lane >> 3), rho = lr & 31;
      const int gr = ISV ? (16 * ((rho >> 3) & 1) + 8 * (rho >> 4) + (rho & 7)) : (2 * (rho & 15) + (rho >> 4));
      const int ch = (lane & 7) ^ ((lr >> 1) & 7);
      srcA[i] = (const char*)(p.x + (size_t)(row0 + (lr & ~31) + gr) * D + ch * 8);
    }
    {
      const int row = lane >> 3, f = ((row >> 1) & 1) | ((w & 1) << 1);
      const int ch = (lane & 7) ^ (f << 1);
      srcB[i] = (const char*)(p.wqkv + (size_t)(w * 8 + row) * LDB + col0 + i * 64 + ch * 8);
    }
  }
  auto issue = [&](int t, int i) {
    int kt = t + rot;
    if (kt >= NKT) kt -= NKT;
    LAS char* sb = lds + (t % NS) * STAGE;
    if (i < 2) glds16<0>(srcA[i] + kt * 128, sb + (w + 8 * i) * 1024);
    else glds16<0>(srcB[i - 2] + (size_t)kt * (64 * LDB * 2), sb + 16384 + (w + 8 * (i - 2)) * 1024);
  };
#pragma unroll
  for (int i = 0; i < 4; i++) issue(0, i);

  int aoff[2], boff[4];
#pragma unroll
  for (int m = 0; m < 2; m++) {
    const int row = wm * 32 + (m ^ wn) * 16 + r16;
    aoff[m] = row * 128 + ((g ^ ((row >> 1) & 7)) << 4);
  }
#pragma unroll
  for (int n = 0; n < 4; n++) {
    const int q = (lane >> 2) & 3, pp = lane & 3, f = ((q >> 1) & 1) | ((g & 1) << 1);
    const int nt = (n + wm) & 3;
    boff[n] = 16384 + wn * 8192 + (g * 8 + q) * 128 + (((nt * 2 + (pp >> 1)) ^ (f << 1)) << 4) + (pp & 1) * 8;
  }
  f32x4 acc[2][4], a_s1 = {0.f, 0.f, 0.f, 0.f}, a_s2 = a_s1, a_cs = a_s1;
#pragma unroll
  for (int m = 0; m < 2; m++)
#pragma unroll
    for (int n = 0; n < 4; n++) acc[m][n] = a_s1;
  const u32x4 ones = {0x3f803f80u, 0x3f803f80u, 0x3f803f80u, 0x3f803f80u};
  u32x4 af[2][2][2];
  u32x2 bq[2][2][4][2];
  auto read1 = [&](int t, int r) {
    LAS char* sb = lds + (t % NS) * STAGE;
    const int bi = t & 1;
    if (r < 8) {
      const int kk = r >> 2, n = r & 3;
      const unsigned ad = (unsigned)(size_t)(sb + boff[n]);
      if (kk == 0) asm volatile("ds_read_b64_tr_b16 %0, %2\n\tds_read_b64_tr_b16 %1, %2 offset:512"
                                : "=&v"(bq[bi][0][n][0]), "=&v"(bq[bi][0][n][1]) : "v"(ad));
      else asm volatile("ds_read_b64_tr_b16 %0, %2 offset:4096\n\tds_read_b64_tr_b16 %1, %2 offset:4608"
                        : "=&v"(bq[bi][1][n][0]), "=&v"(bq[bi][1][n][1]) : "v"(ad));
    } else {
      const int kk = (r - 8) >> 1, m = r & 1;
      af[bi][kk][m] = *(LAS u32x4*)(sb + (aoff[m] ^ (kk << 6)));
    }
  };
#pragma unroll
  for (int i = 0; i < 4; i++) issue(1, i);
  wait_vm<4>();
  bar();
  STAMP(1);
#pragma unroll
  for (int r = 0; r < 12; r++) { read1(0, r); if (r < 8) issue(2 + (r >> 2), r & 3); }
  static_for<0, NKT>([&](auto tc) {
    constexpr int t = tc.value, bi = t & 1;
    if constexpr (t + 1 < NKT) wait_vm<4 * ((NKT - 2 - t) < (NS - 2) ? (NKT - 2 - t) : (NS - 2))>();
    asm volatile("s_waitcnt lgkmcnt(0)"
                 : "+v"(bq[bi][0][0][0]), "+v"(bq[bi][0][0][1]), "+v"(bq[bi][1][0][0]), "+v"(bq[bi][1][0][1]),
                   "+v"(bq[bi][0][1][0]), "+v"(bq[bi][0][1][1]), "+v"(bq[bi][1][1][0]), "+v"(bq[bi][1][1][1]),
                   "+v"(bq[bi][0][2][0]), "+v"(bq[bi][0][2][1]), "+v"(bq[bi][1][2][0]), "+v"(bq[bi][1][2][1]),
                   "+v"(bq[bi][0][3][0]), "+v"(bq[bi][0][3][1]), "+v"(bq[bi][1][3][0]), "+v"(bq[bi][1][3][1]));
    if constexpr (t + 1 < NKT) bar();
    SB();
#pragma unroll
    for (int i = 0; i < 22; i++) {
      const int kk = i / 11, j = i % 11;
      if (j < 8) {
        const int m = j >> 2, n = j & 3;
        const u32x4 bv = {bq[bi][kk][n][0][0], bq[bi][kk][n][0][1], bq[bi][kk][n][1][0], bq[bi][kk][n][1][1]};
        acc[m][n] = ISV ? mma16(af[bi][kk][m], bv, acc[m][n]) : mma16(bv, af[bi][kk][m], acc[m][n]);
      } else if (j == 8) {
        a_s1 = mma16(ones, af[bi][kk][0], a_s1);
      } else if (j == 9) {
        a_s2 = mma16(af[bi][kk][0], af[bi][kk][0], a_s2);
      } else {
        const u32x4 bv = {bq[bi][kk][0][0][0], bq[bi][kk][0][0][1], bq[bi][kk][0][1][0], bq[bi][kk][0][1][1]};
        a_cs = mma16(ones, bv, a_cs);
      }
      if (t + 1 < NKT && i < 18 && (i % 3) != 2) {
        const int r = (i / 3) * 2 + (i % 3);
        read1(t + 1, r < 4 ? r : (r < 6 ? r + 4 : (r < 10 ? r - 2 : r)));
      }
      if (t + NS < NKT && (i % 5) == 2 && i / 5 < 4) issue(t + NS, i / 5);
      SB();
    }
  });
  STAMP(2);
  {
    const float mean = a_s1[0] * (1.f / D);
    const int e = r16 & 3;
    const float s2 = e == 0 ? a_s2[0] : (e == 1 ? a_s2[1] : (e == 2 ? a_s2[2] : a_s2[3]));
    const float var = fmaxf(s2 * (1.f / D) - mean * mean, 0.f);
    if (g == (r16 >> 2)) {
      st_mean[wm * 32 + wn * 16 + r16] = mean;
      st_rstd[wm * 32 + wn * 16 + r16] = __builtin_amdgcn_rsqf(var + 1e-5f);
      st_cs[(wn * 4 + wm) * 16 + r16] = a_cs[0];
    }
    bar();
  }
  const int tile = (row0 >> 5) + wm;
  if (!ISV) {
    const bool isq = cb < 5;
    float mean[2], rs[2];
#pragma unroll
    for (int m = 0; m < 2; m++) {
      mean[m] = st_mean[wm * 32 + (m ^ wn) * 16 + r16];
      rs[m] = st_rstd[wm * 32 + (m ^ wn) * 16 + r16] * (isq ? 0.22360679774997896f * 1.4426950408889634f : 1.f);
    }
#pragma unroll
    for (int n = 0; n < 4; n++) {
      const int lc = (wn * 4 + ((n + wm) & 3)) * 16 + g * 4;
      const f32x4 c4 = *(LAS f32x4*)(st_cs + lc);
      u32x2 o[2];
#pragma unroll
      for (int m = 0; m < 2; m++) {
        o[m][0] = pk2(rs[m] * (acc[m][n][0] - mean[m] * c4[0]), rs[m] * (acc[m][n][1] - mean[m] * c4[1]));
        o[m][1] = pk2(rs[m] * (acc[m][n][2] - mean[m] * c4[2]), rs[m] * (acc[m][n][3] - mean[m] * c4[3]));
      }
      const u32x4 v = wn ? (u32x4){o[1][0], o[1][1], o[0][0], o[0][1]} : (u32x4){o[0][0], o[0][1], o[1][0], o[1][1]};
      const unsigned cc = col0 + lc - (isq ? 0 : D);
      if (isq) {
        st16(c.rs, WS_Q + ((cc >> 2) * S + tile * 32 + 2 * r16) * 8, v);
      } else {
        const unsigned head = cc / 20u, d0 = cc - head * 20u;
        const unsigned t = WS_KV + head * KV_HEAD + tile * KV_TILE + r16 * 16;
        st16(c.rs, t + d0 * 64, v);
        if (d0 == 16) st16(c.rs, t + 1280, (u32x4){0x3f803f80u, 0x00003f80u, 0x3f803f80u, 0x00003f80u});
      }
    }
  } else {
    f32x4 mean[2], rs[2];
#pragma unroll
    for (int m = 0; m < 2; m++) {
      mean[m] = *(LAS f32x4*)(st_mean + wm * 32 + (m ^ wn) * 16 + g * 4);
      rs[m] = *(LAS f32x4*)(st_rstd + wm * 32 + (m ^ wn) * 16 + g * 4);
    }
#pragma unroll
    for (int n = 0; n < 4; n++) {
      const int lc = (wn * 4 + ((n + wm) & 3)) * 16 + r16;
      const float cs = st_cs[lc];
      u32x2 o[2];
#pragma unroll
      for (int m = 0; m < 2; m++) {
        o[m][0] = pk2(rs[m][0] * (acc[m][n][0] - mean[m][0] * cs), rs[m][1] * (acc[m][n][1] - mean[m][1] * cs));
        o[m][1] = pk2(rs[m][2] * (acc[m][n][2] - mean[m][2] * cs), rs[m][3] * (acc[m][n][3] - mean[m][3] * cs));
      }
      const u32x4 v = wn ? (u32x4){o[1][0], o[1][1], o[0][0], o[0][1]} : (u32x4){o[0][0], o[0][1], o[1][0], o[1][1]};
      const unsigned cc = col0 + lc - 2 * D;
      const unsigned head = cc / 20u, d = cc - head * 20u;
      const unsigned t = WS_KV + tile * KV_TILE + 1536 + g * 384, odd = head & 1;
      const unsigned row = odd ? (d < 4 ? d + 16 : d - 4) : d, e = (row & 3) ? 0u : 0x3f803f80u;
      const bool mix = row >= 16;
      const u32x4 cv = {e, e, e, e};
      st16(c.rs, t + (mix ? head & ~1u : head) * KV_HEAD + (mix && odd ? row + 4 : row) * 16, mix && odd ? cv : v);
      if (mix) st16(c.rs, t + (head | 1u) * KV_HEAD + (odd ? row : row + 4) * 16, odd ? v : cv);
    }
  }
}

__device__ __forceinline__ void retile_job(const Params& p, int j, const bf16*& W, unsigned& dst, int& cpr) {
  if (j < 160) { const int ks = j >> 3, blk = j & 7; W = p.wo + ks * 32 * D + blk * 80; dst = WS_WO + (blk * 20 + ks) * 5120; cpr = 10; }
  else if (j < 480) { const int e = j - 160, ks = e >> 4, blk = e & 15; W = p.w1 + ks * 32 * FF + blk * 96; dst = WS_W1 + (blk * 20 + ks) * 6144; cpr = 12; }
  else { const int e = j - 480, ks = e >> 3, blk = e & 7; W = p.w2 + ks * 32 * D + blk * 80; dst = WS_W2 + (blk * 48 + ks) * 5120; cpr = 10; }
}
__device__ __forceinline__ void retile_load(Ctx& c, const Params& p, int j, LAS char* base) {
  const bf16* W; unsigned dst; int cpr;
  retile_job(p, j, W, dst, cpr);
  const int ld = cpr == 12 ? FF : D;
#pragma unroll
  for (int i = 0; i < 6; i++) {
    const int q = i * 64 + c.lane, row = cpr == 12 ? q / 12 : q / 10, ch = q - row * cpr;
    if (i < 5 || cpr == 12) glds16<0>(W + row * ld + ch * 8, base + i * 1024);
  }
}
__device__ __forceinline__ void retile_store(Ctx& c, const Params& p, int j, LAS char* base) {
  const bf16* W; unsigned dst; int cpr;
  retile_job(p, j, W, dst, cpr);
  const int st = cpr * 16, pp = c.lane & 3;
  const unsigned ad = (unsigned)(size_t)base + (c.g * 8 + ((c.lane >> 2) & 3)) * st;
#pragma unroll
  for (int n = 0; n < 6; n++) {
    if (n < 5 || cpr == 12) {
      const int cq = (n >= 4 && cpr == 10) ? 16 + pp : 8 * (n >> 1) + 2 * pp + (n & 1);
      u32x2 r0, r1;
      asm volatile("ds_read_b64_tr_b16 %0, %2\n\tds_read_b64_tr_b16 %1, %3\n\ts_waitcnt lgkmcnt(0)"
                   : "=&v"(r0), "=&v"(r1) : "v"(ad + cq * 8), "v"(ad + cq * 8 + 4 * st) : "memory");
      st16(c.rs, dst + n * 1024 + c.lane * 16, (u32x4){r0[0], r0[1], r1[0], r1[1]});
    }
  }
}

constexpr int L_WO = 61440, L_W1 = 20480, L_CS = 143360;
__device__ __forceinline__ void ld16s(u32x4& v, const char* p) { asm volatile("global_load_dwordx4 %0, %1, off sc1" : "=v"(v) : "v"(p)); }
__device__ __forceinline__ void ld_frag(u32x4& v, const Ctx& c, unsigned soff) {
  asm volatile("buffer_load_dwordx4 %0, %1, %2, %3 offen sc1" : "=v"(v) : "v"(c.lane * 16), "s"(c.rd), "s"(soff));
}
__device__ __forceinline__ void ld16(u32x4& v, const void* p) { asm volatile("global_load_dwordx4 %0, %1, off" : "=v"(v) : "v"(p)); }
__device__ __forceinline__ void ld8(u32x2& v, const void* p) { asm volatile("global_load_dwordx2 %0, %1, off" : "=v"(v) : "v"(p)); }
template <int N> __device__ __forceinline__ void wait_reg(u32x4& v) { asm volatile("s_waitcnt vmcnt(%1)" : "+v"(v) : "n"(N)); }
__device__ __forceinline__ void raise(Ctx& c, unsigned flag) {
  if (c.tid == 0) __hip_atomic_store((u64*)(c.ws + WS_FLAG) + flag, c.token, __ATOMIC_RELAXED, __HIP_MEMORY_SCOPE_AGENT);
}
template <int J, int NT, int STR> __device__ __forceinline__ void rd_b(u32x4& v, const unsigned (&lb)[3]) {
  constexpr int t = J / NT, tt = STR == 10240 ? (t & 15) : t, per = 61440 / STR;
  asm volatile("ds_read_b128 %0, %1 offset:%2" : "=v"(v) : "v"(lb[tt / per]), "n"((tt % per) * STR + (J % NT) * 1024));
}
template <int OFF> __device__ __forceinline__ void wr_b(unsigned ad, const u32x4& v) {
  asm volatile("ds_write_b128 %0, %1 offset:%2" :: "v"(ad), "v"(v), "n"(OFF) : "memory");
}
template <int N> __device__ __forceinline__ void wait_lds(u32x4& v) { asm volatile("s_waitcnt lgkmcnt(%1)" : "+v"(v) : "n"(N)); }
template <int N> __device__ __forceinline__ void wait_lds(u32x2& v) { asm volatile("s_waitcnt lgkmcnt(%1)" : "+v"(v) : "n"(N)); }
#define WIN 8
template <int T, int N> __device__ __forceinline__ void stair() {
  if constexpr (T == 0) __builtin_amdgcn_s_setprio(3);
  if constexpr (T == N * 3 / 8) __builtin_amdgcn_s_setprio(2);
  if constexpr (T == N * 5 / 8) __builtin_amdgcn_s_setprio(1);
  if constexpr (T == N * 7 / 8) __builtin_amdgcn_s_setprio(0);
}
template <int T0, int T1, int NT, int STR, bool FIRST, int JEND, int NA, int R, class WT>
__device__ __forceinline__ void k_steps(f32x4 (&acc)[NT], u32x4 (&a)[NA], u32x4 (&bf)[R], const unsigned (&lb)[3], WT wt) {
  if constexpr (FIRST) static_for<T0 * NT, T0 * NT + R - 1>([&](auto jc) { rd_b<jc.value, NT, STR>(bf[jc.value % R], lb); });
  static_for<T0 * NT, T1 * NT>([&](auto jc) {
    constexpr int j = jc.value, t = j / NT, n = j % NT;
    if constexpr (n == 0) wt(std::integral_constant<int, t>{});
    if constexpr (j + R - 1 < JEND) rd_b<j + R - 1, NT, STR>(bf[(j + R - 1) % R], lb);
    wait_lds<(j + R - 1 < JEND ? R - 1 : JEND - 1 - j)>(bf[j % R]);
    acc[n] = mma16(bf[j % R], a[t], acc[n]);
    SB();
  });
#pragma unroll
  for (int n = 0; n < NT; n++) asm volatile("" : "+v"(acc[n]));
}
template <bool WO>
__device__ __forceinline__ void epi80(Ctx& c, const Params& p, f32x4 (&acc)[5], const u32x4 (&e4)[2], const u32x2& e2, u32x2 (&hres)[5], int rb, int cb) {
  const int wm = c.w & 3, g = c.g, r16 = c.r16;
  LAS f32x4* red = (LAS f32x4*)c.lds + wm * 320 + c.lane;
  bar();
  if (c.w >= 4) {
#pragma unroll
    for (int n = 0; n < 5; n++) red[n * 64] = acc[n];
  }
  bar();
  if (c.w < 4) {
    const u32x2 ev[5] = {{e4[0][0], e4[0][1]}, {e4[0][2], e4[0][3]}, {e4[1][0], e4[1][1]}, {e4[1][2], e4[1][3]}, e2};
    u32x2 o[5];
#pragma unroll
    for (int n = 0; n < 5; n++) {
      f32x4 v = acc[n] + red[n * 64];
      v[0] += bflo(ev[n][0]); v[1] += bfhi(ev[n][0]); v[2] += bflo(ev[n][1]); v[3] += bfhi(ev[n][1]);
      if (!WO) { v[0] += bflo(hres[n][0]); v[1] += bfhi(hres[n][0]); v[2] += bflo(hres[n][1]); v[3] += bfhi(hres[n][1]); }
      o[n] = (u32x2){pk2(v[0], v[1]), pk2(v[2], v[3])};
    }
    const int row = rb * 64 + wm * 16 + r16;
    if (WO) {
      const int row16 = row >> 4;
#pragma unroll
      for (int q = 0; q < 2; q++) {
        const int c8 = cb * 10 + q * 4 + g;
        st16(c.rs, WS_H + ((row16 * 20 + (c8 >> 2)) * 64 + (c8 & 3) * 16 + r16) * 16, (u32x4){o[2 * q][0], o[2 * q][1], o[2 * q + 1][0], o[2 * q + 1][1]});
      }
      const int c8 = cb * 10 + 8 + (g >> 1);
      const auto x0 = __builtin_amdgcn_permlane16_swap(o[4][0], o[4][0], false, false);
      const auto x1 = __builtin_amdgcn_permlane16_swap(o[4][1], o[4][1], false, false);
      if (!(g & 1)) st16(c.rs, WS_H + ((row16 * 20 + (c8 >> 2)) * 64 + (c8 & 3) * 16 + r16) * 16, (u32x4){x0[0], x1[0], x0[1], x1[1]});
      float s1 = 0.f, s2 = 0.f;
#pragma unroll
      for (int n = 0; n < 5; n++) {
        hres[n] = o[n];
        const float a0 = bflo(o[n][0]), a1 = bfhi(o[n][0]), a2 = bflo(o[n][1]), a3 = bfhi(o[n][1]);
        s1 += (a0 + a1) + (a2 + a3);
        s2 += (a0 * a0 + a1 * a1) + (a2 * a2 + a3 * a3);
      }
      s1 += __shfl_xor(s1, 16); s2 += __shfl_xor(s2, 16);
      s1 += __shfl_xor(s1, 32); s2 += __shfl_xor(s2, 32);
      if (g == 0) st8(c.rs, WS_SP + row16 * 1024 + cb * 128 + r16 * 8, (u32x2){__builtin_bit_cast(unsigned, s1), __builtin_bit_cast(unsigned, s2)});
    } else {
      bf16* op = p.out + (size_t)row * D + cb * 80;
      *(u32x4*)(op + g * 8) = (u32x4){o[0][0], o[0][1], o[1][0], o[1][1]};
      *(u32x4*)(op + 32 + g * 8) = (u32x4){o[2][0], o[2][1], o[3][0], o[3][1]};
      *(u32x2*)(op + 64 + g * 4) = o[4];
    }
  }
}

__device__ __forceinline__ void tail_phases(Ctx& c, const Params& p, int xcd, int i, unsigned att_flag, u32x4 (&stash)[20]) {
  LAS char* lds = c.lds;
  const int lane = c.lane, w = c.w, r16 = c.r16, g = c.g;
  const int wm = w & 3, wk = w >> 2;
  const int rb64 = xcd * 4 + (i & 3), cb80 = i >> 2, rb128 = xcd * 2 + (i & 1), cb96 = i >> 1;
  const unsigned w1g = WS_W1 + cb96 * 122880, w2g = WS_W2 + cb80 * 245760, l16 = lane * 16;
  auto w2_fetch = [&](int slot) {
    const int ts = slot / 10, r = slot - ts * 10, h = r >= 5;
    blds16(c.rs, l16, w2g + ((h * 24 + (ts)) * 5 + r - 5 * h) * 1024, lds + slot * 1024);
  };
  const f32x4 z4 = {0.f, 0.f, 0.f, 0.f};
  u32x4 e4[2];
  u32x2 e2, hres[5];
  rawbar();
  if (w < 6) {
#pragma unroll
    for (int q = 0; q < 6; q++) blds16(c.rs, l16, w1g + ((w) * 6 + q) * 1024, lds + L_W1 + (w * 6 + q) * 1024);
  }
  {
    const bf16* xr = p.x + (size_t)(rb64 * 64 + wm * 16 + r16) * D + cb80 * 80;
    ld16(e4[0], xr + g * 8); ld16(e4[1], xr + 32 + g * 8); ld8(e2, xr + 64 + g * 4);
  }
  if (w < 6) wait_vm<9>(); else wait_vm<3>();
  rawbar();
  raise(c, att_flag);
  STAMP(7);
  await(c, F_ATT + xcd * 32 + lane, 32, 0, 0);
  STAMP(8);
  {
    u32x4 a[10], bf[11];
    const unsigned ag = WS_O + ((rb64 * 4 + wm) * 20 + wk * 10) * 1024;
#pragma unroll
    for (int t = 0; t < 10; t++) ld_frag(a[t], c, ag + (t) * 1024);
    const unsigned l0 = (unsigned)(size_t)lds + L_WO + wk * 51200 + lane * 16, lb[3] = {l0, l0, l0};
    f32x4 acc[5] = {z4, z4, z4, z4, z4};
    k_steps<0, 10, 5, 5120, true, 50>(acc, a, bf, lb, [&](auto tc) { wait_reg<9 - tc.value>(a[tc.value]); });
    STAMP(9);
    asm volatile("" : "+v"(e4[0]), "+v"(e4[1]), "+v"(e2));
    epi80<true>(c, p, acc, e4, e2, hres, rb64, cb80);
  }
  rawbar();
  if (w < 4) wait_vm<0>();
  else if (w < 7) {
#pragma unroll 1
    for (int t = 2 + w; t < 20; t += 3) {
#pragma unroll
      for (int q = 0; q < 6; q++) blds16(c.rs, l16, w1g + ((t) * 6 + q) * 1024, lds + L_W1 + (t * 6 + q) * 1024);
    }
  }
  rawbar();
  raise(c, F_WO + rb64 * 8 + cb80);
  STAMP(10);
  wait_vm<0>();
  rawbar();
  LAS float* st_cs = (LAS float*)(lds + L_CS);
  if (w < 6) {
    LAS char* bl = lds + L_W1 + lane * 16;
    const u32x4 ones = {0x3f803f80u, 0x3f803f80u, 0x3f803f80u, 0x3f803f80u};
    f32x4 cs0 = z4, cs1 = z4;
#pragma unroll
    for (int t = 0; t < 20; t += 2) {
      cs0 = mma16(ones, *(LAS u32x4*)(bl + (t * 6 + w) * 1024), cs0);
      cs1 = mma16(ones, *(LAS u32x4*)(bl + (t * 6 + 6 + w) * 1024), cs1);
    }
    if (g == 0) st_cs[w * 16 + r16] = cs0[0] + cs1[0];
    asm volatile("s_waitcnt lgkmcnt(0)" ::: "memory");
  }
  await(c, F_WO + rb128 * 16 + lane, 16, 0, 0);
  STAMP(11);
  {
    u32x4 a[20], bf[12], bv[3];
    u32x2 sp0, sp1;
    const int row16 = rb128 * 8 + w;
    {
      const char* q = c.ws + WS_SP + row16 * 1024 + g * 256 + r16 * 8;
      asm volatile("global_load_dwordx2 %0, %2, off sc1\n\tglobal_load_dwordx2 %1, %2, off offset:128 sc1" : "=&v"(sp0), "=&v"(sp1) : "v"(q));
    }
    const unsigned ag = WS_H + row16 * 20480;
#pragma unroll
    for (int t = 0; t < WIN; t++) ld_frag(a[t], c, ag + t * 1024);
#pragma unroll
    for (int j = 0; j < 3; j++) ld16(bv[j], p.b1 + cb96 * 96 + j * 32 + g * 8);
    LAS char* bl = lds + L_W1 + lane * 16;
    f32x4 acc[6] = {z4, z4, z4, z4, z4, z4};
    const unsigned l0 = (unsigned)(size_t)bl, lb[3] = {l0, l0 + 61440, l0};
    asm volatile("s_waitcnt lgkmcnt(0)" ::: "memory");
    k_steps<0, 20, 6, 6144, true, 120>(acc, a, bf, lb, [&](auto tc) {
      constexpr int t = tc.value;
      if constexpr (t >= 1 && t + WIN - 1 < 20) ld_frag(a[t + WIN - 1], c, ag + (t + WIN - 1) * 1024);
      if constexpr (t >= 14 && t < 18) { const int s = w * 4 + t - 14; w2_fetch(s < 20 ? s : (s < 30 ? s + 130 : 159)); }
      wait_reg<(t < WIN ? 3 : 0) + (t + WIN - 1 < 20 ? WIN - 1 : 19 - t) + (t >= 14 ? (t < 18 ? t - 13 : 4) : 0)>(a[t]);
      stair<t, 20>();
    });
    STAMP(12);
    asm volatile("" : "+v"(sp0), "+v"(sp1), "+v"(bv[0]), "+v"(bv[1]), "+v"(bv[2]));
    const f32x2 q2 = __builtin_bit_cast(f32x2, sp0) + __builtin_bit_cast(f32x2, sp1);
    float s1 = q2[0], s2 = q2[1];
    s1 += __shfl_xor(s1, 16); s2 += __shfl_xor(s2, 16);
    s1 += __shfl_xor(s1, 32); s2 += __shfl_xor(s2, 32);
    const float mean = s1 * (1.f / D);
    const float rs = __builtin_amdgcn_rsqf(fmaxf(s2 * (1.f / D) - mean * mean, 0.f) + 1e-5f);
#pragma unroll
    for (int j = 0; j < 3; j++) {
      u32x4 o;
#pragma unroll
      for (int h = 0; h < 2; h++) {
        const int n = 2 * j + h;
        const f32x4 c4 = *(LAS f32x4*)(st_cs + n * 16 + g * 4);
        const float v0 = fmaxf(rs * (acc[n][0] - mean * c4[0]) + bflo(bv[j][2 * h]), 0.f);
        const float v1 = fmaxf(rs * (acc[n][1] - mean * c4[1]) + bfhi(bv[j][2 * h]), 0.f);
        const float v2 = fmaxf(rs * (acc[n][2] - mean * c4[2]) + bflo(bv[j][2 * h + 1]), 0.f);
        const float v3 = fmaxf(rs * (acc[n][3] - mean * c4[3]) + bfhi(bv[j][2 * h + 1]), 0.f);
        o[2 * h] = pk2(v0 * v0, v1 * v1); o[2 * h + 1] = pk2(v2 * v2, v3 * v3);
      }
      st16(c.rs, WS_G + (row16 * 48 + cb96 * 3 + j) * 1024 + lane * 16, o);
    }
  }
  bar();
  if (w < 5) {
#pragma unroll
    for (int q = 0; q < 10; q++) w2_fetch(20 + w * 10 + q);
  }
  static_for<0, 10>([&](auto qc) { wr_b<qc.value * 1024>((unsigned)(size_t)lds + (70 + w * 10) * 1024 + lane * 16, stash[qc.value]); });
  {
    const bf16* br = p.b2 + cb80 * 80;
    ld16(e4[0], br + g * 8); ld16(e4[1], br + 32 + g * 8); ld8(e2, br + 64 + g * 4);
  }
  if (w < 5) wait_vm<13>(); else wait_vm<3>();
  bar();
  raise(c, F_FF1 + rb128 * 16 + cb96);
  STAMP(13);
  wait_vm<0>();
  await(c, F_FF1 + (rb64 >> 1) * 16 + lane, 16, 0, 0);
  STAMP(14);
  {
    u32x4 a[24], bf[11];
    const unsigned ag = WS_G + ((rb64 * 4 + wm) * 48 + wk * 24) * 1024;
#pragma unroll
    for (int t = 0; t < WIN; t++) ld_frag(a[t], c, ag + t * 1024);
    auto wt = [&](auto tc) {
      constexpr int t = tc.value;
      if constexpr (t >= 1 && t + WIN - 1 < 24) ld_frag(a[t + WIN - 1], c, ag + (t + WIN - 1) * 1024);
      wait_reg<(t + WIN - 1 < 24 ? WIN - 1 : 23 - t)>(a[t]);
      stair<t % 8, 8>();
    };
    const unsigned l0 = (unsigned)(size_t)lds + wk * 5120 + lane * 16, lb[3] = {l0, l0 + 61440, l0 + 122880};
    f32x4 acc[5] = {z4, z4, z4, z4, z4};
    k_steps<0, 8, 5, 10240, true, 80>(acc, a, bf, lb, wt);
    rawbar();
    asm volatile("s_waitcnt lgkmcnt(0)" ::: "memory");
    static_for<0, 10>([&](auto qc) { wr_b<qc.value * 1024>((unsigned)(size_t)lds + w * 10240 + lane * 16, stash[10 + qc.value]); });
    k_steps<8, 16, 5, 10240, false, 80>(acc, a, bf, lb, wt);
    rawbar();
    k_steps<16, 24, 5, 10240, true, 120>(acc, a, bf, lb, wt);
    STAMP(15);
    asm volatile("" : "+v"(e4[0]), "+v"(e4[1]), "+v"(e2));
    epi80<false>(c, p, acc, e4, e2, hres, rb64, cb80);
  }
}

__device__ __forceinline__ f32x16 mma32(u32x4 a, u32x4 b, f32x16 c) {
  return __builtin_amdgcn_mfma_f32_32x32x16_bf16(__builtin_bit_cast(bf16x8, a), __builtin_bit_cast(bf16x8, b), c, 0, 0, 0);
}
__device__ __forceinline__ f32x16 mma32h(u32x2 a, u32x2 b, f32x16 c) {
  return __builtin_amdgcn_mfma_f32_32x32x8bf16_1k(__builtin_bit_cast(s16x4, a), __builtin_bit_cast(s16x4, b), c, 0, 0, 0);
}
__device__ __forceinline__ float max16(const f32x16& s) {
  float m = fmaxf(fmaxf(s[0], s[1]), s[2]);
#pragma unroll
  for (int i = 3; i < 15; i += 2) m = fmaxf(fmaxf(m, s[i]), s[i + 1]);
  return fmaxf(m, s[15]);
}
__device__ __forceinline__ u32x2 set_shift(u32x2 qhi, float m, int hh) {
  const float a = bflo(pk2(m, 0.f));
  const float r = m - a;
  const float b = bflo(pk2(r, 0.f));
  const u32x2 e = {pk2(-a, -b), pk2(b - r, 0.f)};
  return hh ? e : qhi;
}
__device__ __forceinline__ u32x2 exp4(const f32x16& s, int i) {
  const float e0 = __builtin_amdgcn_exp2f(s[i]), e1 = __builtin_amdgcn_exp2f(s[i + 1]);
  const float e2 = __builtin_amdgcn_exp2f(s[i + 2]), e3 = __builtin_amdgcn_exp2f(s[i + 3]);
  return (u32x2){pk2(e0, e1), pk2(e2, e3)};
}

template <int OFF> __device__ __forceinline__ void rd8(u32x2& v, unsigned ad) { asm volatile("ds_read_b64 %0, %1 offset:%2" : "=v"(v) : "v"(ad), "n"(OFF)); }
template <int OFF> __device__ __forceinline__ void rd16(u32x4& v, unsigned ad) { asm volatile("ds_read_b128 %0, %1 offset:%2" : "=v"(v) : "v"(ad), "n"(OFF)); }
__device__ __forceinline__ unsigned local_s(unsigned v) { asm volatile("" : "+s"(v)); return v; }
__device__ __forceinline__ void wait_k(u32x2& a, u32x2& b, u32x2& h, u32x4& v) {
  asm volatile("s_waitcnt lgkmcnt(2)" : "+v"(a), "+v"(b), "+v"(h), "+v"(v));
}
template <class T> __device__ __forceinline__ void pin(T& v) { asm volatile("" : "+v"(v)); }
__device__ __forceinline__ void ld_flags(u32x4& a, u32x4& b, const char* fp) {
  asm volatile("global_load_dwordx4 %0, %2, off sc1\n\tglobal_load_dwordx4 %1, %2, off offset:16 sc1" : "=&v"(a), "=&v"(b) : "v"(fp));
}

constexpr int SCHED[] = {300, 0, 1, 106, 107, 2, 3, 301, 4, 5, 100, 101, 6, 7, 302, 8, 9, 102, 103, 311, 10, 11, 303, 12, 13, 104, 105, 310, 14, 15};

__device__ __forceinline__ void attn_phase(Ctx& c, const Params& p, int head, int qb, int i, u32x4 (&stash)[20]) {
  LAS char* lds = c.lds;
  const int lane = c.lane, w = c.w;
  const int r = lane & 31, hh = lane >> 5;
  const int qrow = qb * 256 + w * 32 + r;
  const unsigned kvg = WS_KV + head * KV_HEAD;
  const char* qp = c.ws + WS_Q + (size_t)(head * 5 * S + qrow) * 8;
  u32x2 qa, qc, qhi0;
  asm volatile("global_load_dwordx2 %0, %1, off sc1" : "=v"(qa) : "v"(qp + (size_t)(2 * hh) * S * 8));
  asm volatile("global_load_dwordx2 %0, %1, off sc1" : "=v"(qc) : "v"(qp + (size_t)(2 * hh + 1) * S * 8));
  asm volatile("global_load_dwordx2 %0, %1, off sc1" : "=v"(qhi0) : "v"(qp + (size_t)4 * S * 8));
  auto issue1 = [&](int s, int j) {
    blds16(c.rs, lane * 16, kvg + s * AT_STAGE + (w + 8 * j) * 1024, lds + (s % 3) * AT_STAGE + (w + 8 * j) * 1024);
  };
  const int job = blockIdx.x * 3 + w;
  LAS char* jl = lds + 2 * AT_STAGE + w * 6144;
  issue1(0, 0); issue1(0, 1);
  wait_vm<0>();
  asm volatile("" : "+v"(qa), "+v"(qc), "+v"(qhi0));
  const u32x4 qlo = {qa[0], qa[1], qc[0], qc[1]};
  bar();
  STAMP(5);
  issue1(0, 2); issue1(0, 3); issue1(0, 4); issue1(0, 5);
  if (w < 3) retile_load(c, p, job, jl);

  const int koff = hh * 512 + r * 8, khoff = 1024 + lane * 8, voff = 1536 + hh * 384 + (r <= 20 ? r : 21) * 16;
  const u32x2 qz = hh ? (u32x2){0u, 0u} : qhi0;
  const f32x16 zero = {0.f, 0.f, 0.f, 0.f, 0.f, 0.f, 0.f, 0.f, 0.f, 0.f, 0.f, 0.f, 0.f, 0.f, 0.f, 0.f};
  u32x2 qhi;
  f32x16 sc;
  {
    const u32x2 k0a = *(LAS u32x2*)(lds + koff), k0b = *(LAS u32x2*)(lds + koff + 256);
    const u32x4 k0 = {k0a[0], k0a[1], k0b[0], k0b[1]};
    const u32x2 kh0 = *(LAS u32x2*)(lds + khoff);
    const f32x16 s0 = mma32(k0, qlo, zero);
    float m = max16(mma32h(kh0, qz, s0));
    m = fmaxf(m, __shfl_xor(m, 32));
    qhi = set_shift(qhi0, m, hh);
    sc = mma32h(kh0, qhi, s0);
  }
  f32x16 o = zero;
  const unsigned l0 = (unsigned)(size_t)lds;
  u32x2 ka, kb, khi;
  u32x4 v0, v1;
  rd8<KV_TILE>(ka, l0 + koff); rd8<KV_TILE + 256>(kb, l0 + koff); rd8<KV_TILE>(khi, l0 + khoff);
  rd16<0>(v0, l0 + voff); rd16<768>(v1, l0 + voff);
  u32x4 p1p = {0u, 0u, 0u, 0u}, v1p = p1p, fr0 = p1p, fr1 = p1p;
  const int cb80 = i >> 2;
  unsigned ak[2], ah[2], av[2];
  float el[4] = {0.f, 0.f, 0.f, 0.f};
  auto slot = [&](auto tc, auto lc) {
    constexpr int t = tc.value;
    constexpr bool LAST = lc.value;
    constexpr int xk = t + 2 >= 16, ok = ((t + 2) & 15) * KV_TILE, xv = t + 1 >= 16, ov = ((t + 1) & 15) * KV_TILE;
    if constexpr (t == 14 && !LAST) __builtin_amdgcn_s_setprio(3);
    if constexpr (t == 4) __builtin_amdgcn_s_setprio(2);
    if constexpr (t == 9) __builtin_amdgcn_s_setprio(1);
    if constexpr (t == 12) __builtin_amdgcn_s_setprio(0);
    f32x16 sn;
    float e[16];
    unsigned pp[6];
    static_for<0, sizeof(SCHED) / sizeof(int)>([&](auto oc) {
      constexpr int op = SCHED[oc.value];
      if constexpr (op < 16) { e[op] = __builtin_amdgcn_exp2f(sc[op]); if constexpr (op >= 12) pin(e[op]); }
      else if constexpr (op < 106) { pp[op - 100] = pk2(e[2 * (op - 100)], e[2 * (op - 100) + 1]); pin(pp[op - 100]); }
      else if constexpr (op < 108) p1p[op - 104] = pk2(el[2 * (op - 106)], el[2 * (op - 106) + 1]);
      else if constexpr (op == 300) {
        sn = mma32((u32x4){ka[0], ka[1], kb[0], kb[1]}, qlo, zero);
      } else if constexpr (op == 301) {
        sn = mma32h(khi, qhi, sn);
        SB();
        rd8<ok>(ka, ak[xk]); rd8<ok + 256>(kb, ak[xk]); rd8<ok>(khi, ah[xk]);
      } else if constexpr (op == 302) {
        o = mma32(v1p, p1p, o);
      } else if constexpr (op == 310) {
        wait_k(ka, kb, khi, v1p);
      } else if constexpr (op == 311) {
        wait_lds<4>(v0);
      } else {
        o = mma32(v0, (u32x4){pp[0], pp[1], pp[2], pp[3]}, o);
        SB();
        v1p = v1;
        rd16<ov>(v0, av[xv]); rd16<ov + 768>(v1, av[xv]);
      }
      SB();
    });
    p1p[0] = pp[4]; p1p[1] = pp[5];
    el[0] = e[12]; el[1] = e[13]; el[2] = e[14]; el[3] = e[15];
    sc = sn;
  };
  auto warm = [&](unsigned voff, unsigned soff) {
    __builtin_amdgcn_raw_ptr_buffer_load_lds(c.rs, (LAS void*)(lds + 159744), 4, voff, soff, 0, 16);
  };
  const unsigned w1h = WS_W1 + (i >> 1) * 122880 + (i & 1) * 61440, w2p = WS_W2 + cb80 * 245760;
  const unsigned w2w = w2p + w * 5120;
  const unsigned w1v = w * 8192 + lane * 128 < 61440 ? w * 8192 + lane * 128 : 0;
  __builtin_amdgcn_s_setprio(3);
  wait_k(ka, kb, khi, v1p);
#pragma unroll 1
  for (int s = 0; s < 2; s++) {
    const unsigned cb = l0 + s * AT_STAGE, nb = cb + AT_STAGE;
    ak[0] = cb + koff; ak[1] = nb + koff; ah[0] = cb + khoff; ah[1] = nb + khoff; av[0] = cb + voff; av[1] = nb + voff;
    static_for<0, 16>([&](auto tc) {
      constexpr int t = tc.value;
      if constexpr (t < 6) { const unsigned o8 = local_s(s + 1) * AT_STAGE + (w + 8 * t) * 1024; blds16(c.rs, lane * 16, kvg + o8, lds + o8); }
      if constexpr (t == 3) { if (s == 0) { if (w < 3) wait_vm<9>(); else wait_vm<4>(); rawbar(); } }
      if constexpr (t == 9) { if (s == 0 && w < 3) { wait_vm<0>(); retile_store(c, p, local_s(job), jl); } }
      if constexpr (t == 9) { if (s == 1 && w == 7) ld_flags(fr0, fr1, c.ws + WS_FLAG + (F_RT + lane * 4) * 8); }
      if constexpr (t == 14) {
        CYC3(23);
        wait_vm<0>();
        if (s == 1 && w == 7) {
          wait_reg<0>(fr0); wait_reg<0>(fr1);
          const unsigned lo = (unsigned)c.token, hi = (unsigned)(c.token >> 32);
          if (!__all(fr0[0] == lo && fr0[1] == hi && fr0[2] == lo && fr0[3] == hi && fr1[0] == lo && fr1[1] == hi && fr1[2] == lo && fr1[3] == hi)) {
            spin_flags(c, F_RT + lane, 64, F_RT + 64 + lane, 64);
            spin_flags(c, F_RT + 128 + lane, 64, F_RT + 192 + lane, 64);
          }
        }
        rawbar();
        CYC3(24);
        if (s == 0) raise(c, F_RT + blockIdx.x);
      }
      slot(tc, std::false_type{});
    });
  }
  ak[0] = ak[1]; ah[0] = ah[1]; av[0] = av[1];
  ak[1] = l0 + koff; ah[1] = l0 + khoff; av[1] = l0 + voff;
  static_for<0, 16>([&](auto tc) {
    constexpr int t = tc.value;
    if constexpr (t < 6) issue1(3, t);
    else {
      constexpr int q0 = 2 * (t - 6), q1 = q0 + 1;
      ld_frag(stash[q0], c, w2w + (((q0 / 5 & 1) * 24 + (q0 < 10 ? 7 : 16)) * 5 + q0 % 5) * 1024);
      ld_frag(stash[q1], c, w2w + (((q1 / 5 & 1) * 24 + (q1 < 10 ? 7 : 16)) * 5 + q1 % 5) * 1024);
      if constexpr (t % 2 == 1 && t < 14) { if (w == (t - 7) / 2) warm(w1v, w1h); }
    }
    if constexpr (t == 14) { CYC(27); wait_vm<18>(); rawbar(); CYC(28); }
    SB();
    slot(tc, std::false_type{});
  });
  ak[0] = ak[1]; ah[0] = ah[1]; av[0] = av[1];
  static_for<0, 16>([&](auto tc) {
    constexpr int t = tc.value;
    if constexpr (t == 2) { CYC(29); wait_vm<0>(); rawbar(); CYC(30); }
    if constexpr (t >= 2 && t < 15) {
      if (t < 14 || w < 4) blds16(c.rs, lane * 16, WS_WO + cb80 * 102400 + (w + 8 * (t - 2)) * 1024, lds + L_WO + (w + 8 * (t - 2)) * 1024);
      if constexpr (t % 3 == 0) { if (w == 3 + t / 3) __builtin_amdgcn_raw_ptr_buffer_load_lds(c.rs, (LAS void*)(lds + AT_STAGE + w * 256), 4, w1v, w1h, 0, 16); }
      if constexpr (t % 3 == 1 && t < 13) { if (w == t / 3 - 1) __builtin_amdgcn_raw_ptr_buffer_load_lds(c.rs, (LAS void*)(lds + AT_STAGE + w * 256), 4, (w * 64 + lane < 140 ? w * 64 + lane : 0) * 128, w2p + (i & 1) * 122880 + ((i >> 1) & 1) * 17920, 0, 16); }
    }
    SB();
    slot(tc, std::true_type{});
  });
  __builtin_amdgcn_s_setprio(0);
  wait_lds<0>(v1p); wait_lds<0>(ka); wait_lds<0>(kb); wait_lds<0>(khi); wait_lds<0>(v0); wait_lds<0>(v1);
  p1p[2] = pk2(el[0], el[1]); p1p[3] = pk2(el[2], el[3]);
  o = mma32(v1p, p1p, o);
  STAMP(6);
  float l = __shfl(o[8], r + 32);
  if (!__all(l < 1.8446744e19f)) {
    const unsigned kb = WS_KV + head * KV_HEAD;
    auto gk = [&](int t) {
      const u32x2 a = __builtin_amdgcn_raw_buffer_load_b64(c.rs, kb + t * KV_TILE + koff, 0, 16);
      const u32x2 b = __builtin_amdgcn_raw_buffer_load_b64(c.rs, kb + t * KV_TILE + koff + 256, 0, 16);
      return mma32((u32x4){a[0], a[1], b[0], b[1]}, qlo, zero);
    };
    float m = -__builtin_inff();
#pragma unroll 1
    for (int t = 0; t < 64; t++)
      m = fmaxf(m, max16(mma32h(__builtin_amdgcn_raw_buffer_load_b64(c.rs, kb + t * KV_TILE + khoff, 0, 16), qz, gk(t))));
    m = fmaxf(m, __shfl_xor(m, 32));
    qhi = set_shift(qhi0, m, hh);
    o = zero;
#pragma unroll 1
    for (int t = 0; t < 64; t++) {
      const f32x16 s2 = mma32h(__builtin_amdgcn_raw_buffer_load_b64(c.rs, kb + t * KV_TILE + khoff, 0, 16), qhi, gk(t));
      const u32x2 pa = exp4(s2, 0), pb = exp4(s2, 4), pc = exp4(s2, 8), pd = exp4(s2, 12);
      o = mma32(__builtin_amdgcn_raw_buffer_load_b128(c.rs, kb + t * KV_TILE + voff, 0, 16), (u32x4){pa[0], pa[1], pb[0], pb[1]}, o);
      o = mma32(__builtin_amdgcn_raw_buffer_load_b128(c.rs, kb + t * KV_TILE + voff + 768, 0, 16), (u32x4){pc[0], pc[1], pd[0], pd[1]}, o);
    }
    l = __shfl(o[8], r + 32);
  }
  const float inv = 1.f / l;
  const auto x0 = __builtin_amdgcn_permlane32_swap(pk2(o[0] * inv, o[1] * inv), pk2(o[4] * inv, o[5] * inv), false, false);
  const auto x1 = __builtin_amdgcn_permlane32_swap(pk2(o[2] * inv, o[3] * inv), pk2(o[6] * inv, o[7] * inv), false, false);
  const int ca = head * DH + (head & 1) * 4 + hh * 8, cr = head * DH + ((head & 1) ? 0 : 16);
  const unsigned ob = WS_O + (qrow >> 4) * 20480 + (qrow & 15) * 16;
  st16(c.rs, ob + (ca >> 3) * 256, (u32x4){x0[0], x1[0], x0[1], x1[1]});
  if (hh == 0) st8(c.rs, ob + (cr >> 3) * 256 + (cr & 4) * 2, (u32x2){pk2(o[8] * inv, o[9] * inv), pk2(o[10] * inv, o[11] * inv)});
}

__global__ __launch_bounds__(512, 2) void k_block(Params p) {
  extern __shared__ __attribute__((aligned(16))) char smem[];
  Ctx c;
  c.tid = threadIdx.x; c.lane = c.tid & 63; c.w = __builtin_amdgcn_readfirstlane(c.tid >> 6);
  c.r16 = c.lane & 15; c.g = c.lane >> 4;
  c.lds = (LAS char*)smem; c.ws = p.ws;
  c.rs = __builtin_amdgcn_make_buffer_rsrc(p.ws, 0, WS_END, 0x00020000);
  c.rd = (u32x4){(unsigned)(size_t)p.ws, (unsigned)((size_t)p.ws >> 32) & 0xffffu, WS_END, 0x00020000u};
  STAMP(0);
  if (c.w == 7) {
    __builtin_amdgcn_raw_ptr_buffer_load_lds(c.rs, (LAS void*)(c.lds + 150016), 4, (c.lane * 6 + (blockIdx.x >> 3) % 6) * 65536, 0, 0, 0);
    const char* q = c.lane < 20 ? (const char*)p.out + c.lane * 131072 : c.lane < 27 ? (const char*)p.wo + (c.lane - 20) * 131072 : c.lane < 42 ? (const char*)p.w1 + (c.lane - 27) * 131072 : c.lane < 57 ? (const char*)p.w2 + (c.lane - 42) * 131072 : (const char*)p.b1;
    __builtin_amdgcn_global_load_lds((const GAS void*)q, (LAS void*)(c.lds + 150528), 4, 0, 0);
  }
  const u64 epoch = __hip_atomic_load((const u64*)(p.ws + WS_FLAG) + F_EPOCH, __ATOMIC_RELAXED, __HIP_MEMORY_SCOPE_AGENT);
  const int b = blockIdx.x, xcd = b & 7, i = b >> 3;
  {
    const int rb = (xcd & 3) * 4 + (i & 3), cb = (xcd >> 2) * 8 + (i >> 2);
    if (cb < 10) qkv_phase<false>(c, p, rb, cb);
    else if (cb < 15) qkv_phase<true>(c, p, rb, cb);
    else if (c.w < 6) {
      retile_load(c, p, 768 + rb * 6 + c.w, c.lds + c.w * 6144);
      wait_vm<0>();
      retile_store(c, p, 768 + rb * 6 + c.w, c.lds + c.w * 6144);
    }
    u64 z = (epoch + 0x9e3779b97f4a7c15ull) * 0xbf58476d1ce4e5b9ull;
    c.token = z ^ (z >> 31);
    if (cb < 15) publish(c, F_QKV + cb * 16 + rb);
    STAMP(3);
  }
  const int head = xcd * 4 + (i & 3), qb = i >> 2;
  {
    const int c0 = head * 20 / 128, c1 = (head * 20 + 19) / 128, j = c.lane >> 4;
    await(c, F_QKV + (((j & 1) ? c1 : c0) + (j >> 1) * 5) * 16 + c.r16, 64, F_QKV + (((j & 1) ? c1 : c0) + 10) * 16 + c.r16, 32);
  }
  STAMP(4);
  u32x4 stash[20];
  attn_phase(c, p, head, qb, i, stash);
  tail_phases(c, p, xcd, i, F_ATT + qb * 32 + head, stash);
  if (b == 0 && c.tid == 0) __hip_atomic_store((u64*)(p.ws + WS_FLAG) + F_EPOCH, epoch + 1, __ATOMIC_RELAXED, __HIP_MEMORY_SCOPE_AGENT);
}

extern "C" void kernel_launch(void* const* d_in, const int* in_sizes, int n_in, void* d_out, int out_size, void* d_ws,
                              size_t ws_size, hipStream_t stream) {
  Params p;
  p.x = (const bf16*)d_in[0]; p.wqkv = (const bf16*)d_in[1]; p.wo = (const bf16*)d_in[2];
  p.w1 = (const bf16*)d_in[4]; p.b1 = (const bf16*)d_in[5]; p.w2 = (const bf16*)d_in[6]; p.b2 = (const bf16*)d_in[7];
  p.out = (bf16*)d_out; p.ws = (char*)d_ws;
  (void)hipFuncSetAttribute((const void*)k_block, hipFuncAttributeMaxDynamicSharedMemorySize, LDS_BYTES);
  k_block<<<256, 512, LDS_BYTES, stream>>>(p);
}
```

```cpp
#include <hip/hip_runtime.h>
#include <hip/hip_bf16.h>
#include <stdint.h>
#include <type_traits>

#define LAS __attribute__((address_space(3)))
#define GAS __attribute__((address_space(1)))
typedef __bf16 bf16;
typedef unsigned long long u64;
typedef __bf16 bf16x2 __attribute__((ext_vector_type(2)));
typedef __bf16 bf16x8 __attribute__((ext_vector_type(8)));
typedef short s16x4 __attribute__((ext_vector_type(4)));
typedef float f32x2 __attribute__((ext_vector_type(2)));
typedef float f32x4 __attribute__((ext_vector_type(4)));
typedef float f32x16 __attribute__((ext_vector_type(16)));
typedef unsigned u32x2 __attribute__((ext_vector_type(2)));
typedef unsigned u32x4 __attribute__((ext_vector_type(4)));
typedef __amdgpu_buffer_rsrc_t rsrc_t;

constexpr int S = 2048, D = 640, NH = 32, DH = 20, FF = 1536;
constexpr int KV_TILE = 3072;
constexpr int KV_HEAD = 64 * KV_TILE;
constexpr int AT_STAGE = 16 * KV_TILE;
constexpr int LDS_BYTES = 163840;
constexpr unsigned WS_FLAG = 0;
constexpr unsigned F_QKV = 0, F_ATT = 256, F_WO = 512, F_FF1 = 768, F_RT = 1024, F_EPOCH = 1536;
constexpr unsigned WS_Q = 16384;
constexpr unsigned WS_KV = WS_Q + NH * S * 40;
constexpr unsigned WS_O = WS_KV + NH * KV_HEAD;
constexpr unsigned WS_H = WS_O + S * D * 2;
constexpr unsigned WS_G = WS_H + S * D * 2;
constexpr unsigned WS_WO = WS_G + S * FF * 2;
constexpr unsigned WS_W1 = WS_WO + D * D * 2;
constexpr unsigned WS_W2 = WS_W1 + D * FF * 2;
constexpr unsigned WS_SP = WS_W2 + D * FF * 2;
constexpr unsigned WS_END = WS_SP + S * 64;

#define SB() __builtin_amdgcn_sched_barrier(0)
template <int N> __device__ __forceinline__ void wait_vm() { asm volatile("s_waitcnt vmcnt(%0)" ::"n"(N) : "memory"); }
__device__ __forceinline__ void bar() { asm volatile("s_waitcnt lgkmcnt(0)" ::: "memory"); __builtin_amdgcn_s_barrier(); asm volatile("" ::: "memory"); }
__device__ __forceinline__ void rawbar() { asm volatile("" ::: "memory"); __builtin_amdgcn_s_barrier(); asm volatile("" ::: "memory"); }
template <int AUX> __device__ __forceinline__ void glds16(const void* g, LAS char* l) {
  __builtin_amdgcn_global_load_lds((const GAS void*)g, (LAS void*)l, 16, 0, AUX);
}
__device__ __forceinline__ void blds16(rsrc_t r, unsigned voff, unsigned soff, LAS char* l) {
  __builtin_amdgcn_raw_ptr_buffer_load_lds(r, (LAS void*)l, 16, voff, soff, 0, 16);
}
__device__ __forceinline__ void st16(rsrc_t r, unsigned off, u32x4 v) { __builtin_amdgcn_raw_buffer_store_b128(v, r, off, 0, 16); }
__device__ __forceinline__ void st8(rsrc_t r, unsigned off, u32x2 v) { __builtin_amdgcn_raw_buffer_store_b64(v, r, off, 0, 16); }
__device__ __forceinline__ unsigned pk2(float a, float b) {
  f32x2 f = {a, b};
  return __builtin_bit_cast(unsigned, __builtin_convertvector(f, bf16x2));
}
__device__ __forceinline__ float bflo(unsigned u) { return __builtin_bit_cast(float, u << 16); }
__device__ __forceinline__ float bfhi(unsigned u) { return __builtin_bit_cast(float, u & 0xffff0000u); }
__device__ __forceinline__ f32x4 mma16(u32x4 a, u32x4 b, f32x4 c) {
  return __builtin_amdgcn_mfma_f32_16x16x32_bf16(__builtin_bit_cast(bf16x8, a), __builtin_bit_cast(bf16x8, b), c, 0, 0, 0);
}
template <int I, int N, class F> __device__ __forceinline__ void static_for(F&& f) {
  if constexpr (I < N) { f(std::integral_constant<int, I>{}); static_for<I + 1, N>(f); }
}

struct Params {
  const bf16 *x, *wqkv, *wo, *w1, *b1, *w2, *b2;
  bf16* out;
  char* ws;
};
struct Ctx {
  int tid, lane, w, r16, g;
  LAS char* lds;
  char* ws;
  rsrc_t rs;
  u32x4 rd;
  u64 token;
};
#define CYC(i) do {} while (0)
#define CYC3(i) do {} while (0)
#define STAMP(i) do {} while (0)

__device__ __forceinline__ void publish(Ctx& c, unsigned flag) {
  wait_vm<0>();
  bar();
  if (c.tid == 0) __hip_atomic_store((u64*)(c.ws + WS_FLAG) + flag, c.token, __ATOMIC_RELAXED, __HIP_MEMORY_SCOPE_AGENT);
}
__device__ __forceinline__ void spin_flags(Ctx& c, unsigned idx0, int n0, unsigned idx1, int n1) {
  const u64* f = (const u64*)(c.ws + WS_FLAG);
  for (int it = 0; it < (1 << 20); it++) {
    u64 a = c.token, b = c.token;
    if (c.lane < n0) a = __hip_atomic_load(f + idx0, __ATOMIC_RELAXED, __HIP_MEMORY_SCOPE_AGENT);
    if (c.lane < n1) b = __hip_atomic_load(f + idx1, __ATOMIC_RELAXED, __HIP_MEMORY_SCOPE_AGENT);
    if (__all(a == c.token && b == c.token)) break;
    __builtin_amdgcn_s_sleep(1);
  }
}
__device__ __forceinline__ void await(Ctx& c, unsigned idx0, int n0, unsigned idx1, int n1) {
  if (c.w == 7) spin_flags(c, idx0, n0, idx1, n1);
  rawbar();
}

template <bool ISV>
__device__ __forceinline__ void qkv_phase(Ctx& c, const Params& p, int rb, int cb) {
  constexpr int NS = 4, NKT = 10, STAGE = 32768, LDB = 3 * D;
  LAS char* lds = c.lds;
  LAS float* st_mean = (LAS float*)(lds + NS * STAGE);
  LAS float* st_rstd = st_mean + 128;
  LAS float* st_cs = st_rstd + 128;
  const int lane = c.lane, w = c.w, r16 = c.r16, g = c.g;
  const int wm = w & 3, wn = w >> 2;
  const int row0 = rb * 128, col0 = cb * 128;
  const int rot = (rb + cb) % NKT;

  const char* srcA[2];
  const char* srcB[2];
#pragma unroll
  for (int i = 0; i < 2; i++) {
    const int pc = w + 8 * i;
    {
      const int lr = pc * 8 + (lane >> 3), rho = lr & 31;
      const int gr = ISV ? (16 * ((rho >> 3) & 1) + 8 * (rho >> 4) + (rho & 7)) : (2 * (rho & 15) + (rho >> 4));
      const int ch = (lane & 7) ^ ((lr >> 1) & 7);
      srcA[i] = (const char*)(p.x + (size_t)(row0 + (lr & ~31) + gr) * D + ch * 8);
    }
    {
      const int row = lane >> 3, f = ((row >> 1) & 1) | ((w & 1) << 1);
      const int ch = (lane & 7) ^ (f << 1);
      srcB[i] = (const char*)(p.wqkv + (size_t)(w * 8 + row) * LDB + col0 + i * 64 + ch * 8);
    }
  }
  auto issue = [&](int t, int i) {
    int kt = t + rot;
    if (kt >= NKT) kt -= NKT;
    LAS char* sb = lds + (t % NS) * STAGE;
    if (i < 2) glds16<0>(srcA[i] + kt * 128, sb + (w + 8 * i) * 1024);
    else glds16<0>(srcB[i - 2] + (size_t)kt * (64 * LDB * 2), sb + 16384 + (w + 8 * (i - 2)) * 1024);
  };
#pragma unroll
  for (int i = 0; i < 4; i++) issue(0, i);

  int aoff[2], boff[4];
#pragma unroll
  for (int m = 0; m < 2; m++) {
    const int row = wm * 32 + (m ^ wn) * 16 + r16;
    aoff[m] = row * 128 + ((g ^ ((row >> 1) & 7)) << 4);
  }
#pragma unroll
  for (int n = 0; n < 4; n++) {
    const int q = (lane >> 2) & 3, pp = lane & 3, f = ((q >> 1) & 1) | ((g & 1) << 1);
    const int nt = (n + wm) & 3;
    boff[n] = 16384 + wn * 8192 + (g * 8 + q) * 128 + (((nt * 2 + (pp >> 1)) ^ (f << 1)) << 4) + (pp & 1) * 8;
  }
  f32x4 acc[2][4], a_s1 = {0.f, 0.f, 0.f, 0.f}, a_s2 = a_s1, a_cs = a_s1;
#pragma unroll
  for (int m = 0; m < 2; m++)
#pragma unroll
    for (int n = 0; n < 4; n++) acc[m][n] = a_s1;
  const u32x4 ones = {0x3f803f80u, 0x3f803f80u, 0x3f803f80u, 0x3f803f80u};
  u32x4 af[2][2][2];
  u32x2 bq[2][2][4][2];
  auto read1 = [&](int t, int r) {
    LAS char* sb = lds + (t % NS) * STAGE;
    const int bi = t & 1;
    if (r < 8) {
      const int kk = r >> 2, n = r & 3;
      const unsigned ad = (unsigned)(size_t)(sb + boff[n]);
      if (kk == 0) asm volatile("ds_read_b64_tr_b16 %0, %2\n\tds_read_b64_tr_b16 %1, %2 offset:512"
                                : "=&v"(bq[bi][0][n][0]), "=&v"(bq[bi][0][n][1]) : "v"(ad));
      else asm volatile("ds_read_b64_tr_b16 %0, %2 offset:4096\n\tds_read_b64_tr_b16 %1, %2 offset:4608"
                        : "=&v"(bq[bi][1][n][0]), "=&v"(bq[bi][1][n][1]) : "v"(ad));
    } else {
      const int kk = (r - 8) >> 1, m = r & 1;
      af[bi][kk][m] = *(LAS u32x4*)(sb + (aoff[m] ^ (kk << 6)));
    }
  };
#pragma unroll
  for (int i = 0; i < 4; i++) issue(1, i);
  wait_vm<4>();
  bar();
  STAMP(1);
#pragma unroll
  for (int r = 0; r < 12; r++) { read1(0, r); if (r < 8) issue(2 + (r >> 2), r & 3); }
  static_for<0, NKT>([&](auto tc) {
    constexpr int t = tc.value, bi = t & 1;
    if constexpr (t + 1 < NKT) wait_vm<4 * ((NKT - 2 - t) < (NS - 2) ? (NKT - 2 - t) : (NS - 2))>();
    asm volatile("s_waitcnt lgkmcnt(0)"
                 : "+v"(bq[bi][0][0][0]), "+v"(bq[bi][0][0][1]), "+v"(bq[bi][1][0][0]), "+v"(bq[bi][1][0][1]),
                   "+v"(bq[bi][0][1][0]), "+v"(bq[bi][0][1][1]), "+v"(bq[bi][1][1][0]), "+v"(bq[bi][1][1][1]),
                   "+v"(bq[bi][0][2][0]), "+v"(bq[bi][0][2][1]), "+v"(bq[bi][1][2][0]), "+v"(bq[bi][1][2][1]),
                   "+v"(bq[bi][0][3][0]), "+v"(bq[bi][0][3][1]), "+v"(bq[bi][1][3][0]), "+v"(bq[bi][1][3][1]));
    if constexpr (t + 1 < NKT) bar();
    SB();
#pragma unroll
    for (int i = 0; i < 22; i++) {
      const int kk = i / 11, j = i % 11;
      if (j < 8) {
        const int m = j >> 2, n = j & 3;
        const u32x4 bv = {bq[bi][kk][n][0][0], bq[bi][kk][n][0][1], bq[bi][kk][n][1][0], bq[bi][kk][n][1][1]};
        acc[m][n] = ISV ? mma16(af[bi][kk][m], bv, acc[m][n]) : mma16(bv, af[bi][kk][m], acc[m][n]);
      } else if (j == 8) {
        a_s1 = mma16(ones, af[bi][kk][0], a_s1);
      } else if (j == 9) {
        a_s2 = mma16(af[bi][kk][0], af[bi][kk][0], a_s2);
      } else {
        const u32x4 bv = {bq[bi][kk][0][0][0], bq[bi][kk][0][0][1], bq[bi][kk][0][1][0], bq[bi][kk][0][1][1]};
        a_cs = mma16(ones, bv, a_cs);
      }
      if (t + 1 < NKT && i < 18 && (i % 3) != 2) {
        const int r = (i / 3) * 2 + (i % 3);
        read1(t + 1, r < 4 ? r : (r < 6 ? r + 4 : (r < 10 ? r - 2 : r)));
      }
      if (t + NS < NKT && (i % 5) == 2 && i / 5 < 4) issue(t + NS, i / 5);
      SB();
    }
  });
  STAMP(2);
  {
    const float mean = a_s1[0] * (1.f / D);
    const int e = r16 & 3;
    const float s2 = e == 0 ? a_s2[0] : (e == 1 ? a_s2[1] : (e == 2 ? a_s2[2] : a_s2[3]));
    const float var = fmaxf(s2 * (1.f / D) - mean * mean, 0.f);
    if (g == (r16 >> 2)) {
      st_mean[wm * 32 + wn * 16 + r16] = mean;
      st_rstd[wm * 32 + wn * 16 + r16] = __builtin_amdgcn_rsqf(var + 1e-5f);
      st_cs[(wn * 4 + wm) * 16 + r16] = a_cs[0];
    }
    bar();
  }
  const int tile = (row0 >> 5) + wm;
  if (!ISV) {
    const bool isq = cb < 5;
    float mean[2], rs[2];
#pragma unroll
    for (int m = 0; m < 2; m++) {
      mean[m] = st_mean[wm * 32 + (m ^ wn) * 16 + r16];
      rs[m] = st_rstd[wm * 32 + (m ^ wn) * 16 + r16] * (isq ? 0.22360679774997896f * 1.4426950408889634f : 1.f);
    }
#pragma unroll
    for (int n = 0; n < 4; n++) {
      const int lc = (wn * 4 + ((n + wm) & 3)) * 16 + g * 4;
      const f32x4 c4 = *(LAS f32x4*)(st_cs + lc);
      u32x2 o[2];
#pragma unroll
      for (int m = 0; m < 2; m++) {
        o[m][0] = pk2(rs[m] * (acc[m][n][0] - mean[m] * c4[0]), rs[m] * (acc[m][n][1] - mean[m] * c4[1]));
        o[m][1] = pk2(rs[m] * (acc[m][n][2] - mean[m] * c4[2]), rs[m] * (acc[m][n][3] - mean[m] * c4[3]));
      }
      const u32x4 v = wn ? (u32x4){o[1][0], o[1][1], o[0][0], o[0][1]} : (u32x4){o[0][0], o[0][1], o[1][0], o[1][1]};
      const unsigned cc = col0 + lc - (isq ? 0 : D);
      if (isq) {
        st16(c.rs, WS_Q + ((cc >> 2) * S + tile * 32 + 2 * r16) * 8, v);
      } else {
        const unsigned head = cc / 20u, d0 = cc - head * 20u;
        const unsigned t = WS_KV + head * KV_HEAD + tile * KV_TILE + r16 * 16;
        st16(c.rs, t + d0 * 64, v);
        if (d0 == 16) st16(c.rs, t + 1280, (u32x4){0x3f803f80u, 0x00003f80u, 0x3f803f80u, 0x00003f80u});
      }
    }
  } else {
    f32x4 mean[2], rs[2];
#pragma unroll
    for (int m = 0; m < 2; m++) {
      mean[m] = *(LAS f32x4*)(st_mean + wm * 32 + (m ^ wn) * 16 + g * 4);
      rs[m] = *(LAS f32x4*)(st_rstd + wm * 32 + (m ^ wn) * 16 + g * 4);
    }
#pragma unroll
    for (int n = 0; n < 4; n++) {
      const int lc = (wn * 4 + ((n + wm) & 3)) * 16 + r16;
      const float cs = st_cs[lc];
      u32x2 o[2];
#pragma unroll
      for (int m = 0; m < 2; m++) {
        o[m][0] = pk2(rs[m][0] * (acc[m][n][0] - mean[m][0] * cs), rs[m][1] * (acc[m][n][1] - mean[m][1] * cs));
        o[m][1] = pk2(rs[m][2] * (acc[m][n][2] - mean[m][2] * cs), rs[m][3] * (acc[m][n][3] - mean[m][3] * cs));
      }
      const u32x4 v = wn ? (u32x4){o[1][0], o[1][1], o[0][0], o[0][1]} : (u32x4){o[0][0], o[0][1], o[1][0], o[1][1]};
      const unsigned cc = col0 + lc - 2 * D;
      const unsigned head = cc / 20u, d = cc - head * 20u;
      const unsigned t = WS_KV + tile * KV_TILE + 1536 + g * 384, odd = head & 1;
      const unsigned row = odd ? (d < 4 ? d + 16 : d - 4) : d, e = (row & 3) ? 0u : 0x3f803f80u;
      const bool mix = row >= 16;
      const u32x4 cv = {e, e, e, e};
      st16(c.rs, t + (mix ? head & ~1u : head) * KV_HEAD + (mix && odd ? row + 4 : row) * 16, mix && odd ? cv : v);
      if (mix) st16(c.rs, t + (head | 1u) * KV_HEAD + (odd ? row : row + 4) * 16, odd ? v : cv);
    }
  }
}

__device__ __forceinline__ void retile_job(const Params& p, int j, const bf16*& W, unsigned& dst, int& cpr) {
  if (j < 160) { const int ks = j >> 3, blk = j & 7; W = p.wo + ks * 32 * D + blk * 80; dst = WS_WO + (blk * 20 + ks) * 5120; cpr = 10; }
  else if (j < 480) { const int e = j - 160, ks = e >> 4, blk = e & 15; W = p.w1 + ks * 32 * FF + blk * 96; dst = WS_W1 + (blk * 20 + ks) * 6144; cpr = 12; }
  else { const int e = j - 480, ks = e >> 3, blk = e & 7; W = p.w2 + ks * 32 * D + blk * 80; dst = WS_W2 + (blk * 48 + ks) * 5120; cpr = 10; }
}
__device__ __forceinline__ void retile_load(Ctx& c, const Params& p, int j, LAS char* base) {
  const bf16* W; unsigned dst; int cpr;
  retile_job(p, j, W, dst, cpr);
  const int ld = cpr == 12 ? FF : D;
#pragma unroll
  for (int i = 0; i < 6; i++) {
    const int q = i * 64 + c.lane, row = cpr == 12 ? q / 12 : q / 10, ch = q - row * cpr;
    if (i < 5 || cpr == 12) glds16<0>(W + row * ld + ch * 8, base + i * 1024);
  }
}
__device__ __forceinline__ void retile_store(Ctx& c, const Params& p, int j, LAS char* base) {
  const bf16* W; unsigned dst; int cpr;
  retile_job(p, j, W, dst, cpr);
  const int st = cpr * 16, pp = c.lane & 3;
  const unsigned ad = (unsigned)(size_t)base + (c.g * 8 + ((c.lane >> 2) & 3)) * st;
#pragma unroll
  for (int n = 0; n < 6; n++) {
    if (n < 5 || cpr == 12) {
      const int cq = (n >= 4 && cpr == 10) ? 16 + pp : 8 * (n >> 1) + 2 * pp + (n & 1);
      u32x2 r0, r1;
      asm volatile("ds_read_b64_tr_b16 %0, %2\n\tds_read_b64_tr_b16 %1, %3\n\ts_waitcnt lgkmcnt(0)"
                   : "=&v"(r0), "=&v"(r1) : "v"(ad + cq * 8), "v"(ad + cq * 8 + 4 * st) : "memory");
      st16(c.rs, dst + n * 1024 + c.lane * 16, (u32x4){r0[0], r0[1], r1[0], r1[1]});
    }
  }
}

constexpr int L_WO = 61440, L_W1 = 20480, L_CS = 143360;
__device__ __forceinline__ void ld16s(u32x4& v, const char* p) { asm volatile("global_load_dwordx4 %0, %1, off sc1" : "=v"(v) : "v"(p)); }
__device__ __forceinline__ void ld_frag(u32x4& v, const Ctx& c, unsigned soff) {
  asm volatile("buffer_load_dwordx4 %0, %1, %2, %3 offen sc1" : "=v"(v) : "v"(c.lane * 16), "s"(c.rd), "s"(soff));
}
__device__ __forceinline__ void ld16(u32x4& v, const void* p) { asm volatile("global_load_dwordx4 %0, %1, off" : "=v"(v) : "v"(p)); }
__device__ __forceinline__ void ld8(u32x2& v, const void* p) { asm volatile("global_load_dwordx2 %0, %1, off" : "=v"(v) : "v"(p)); }
template <int N> __device__ __forceinline__ void wait_reg(u32x4& v) { asm volatile("s_waitcnt vmcnt(%1)" : "+v"(v) : "n"(N)); }
__device__ __forceinline__ void raise(Ctx& c, unsigned flag) {
  if (c.tid == 0) __hip_atomic_store((u64*)(c.ws + WS_FLAG) + flag, c.token, __ATOMIC_RELAXED, __HIP_MEMORY_SCOPE_AGENT);
}
template <int J, int NT, int STR> __device__ __forceinline__ void rd_b(u32x4& v, const unsigned (&lb)[3]) {
  constexpr int t = J / NT, tt = STR == 10240 ? (t & 15) : t, per = 61440 / STR;
  asm volatile("ds_read_b128 %0, %1 offset:%2" : "=v"(v) : "v"(lb[tt / per]), "n"((tt % per) * STR + (J % NT) * 1024));
}
template <int OFF> __device__ __forceinline__ void wr_b(unsigned ad, const u32x4& v) {
  asm volatile("ds_write_b128 %0, %1 offset:%2" :: "v"(ad), "v"(v), "n"(OFF) : "memory");
}
template <int N> __device__ __forceinline__ void wait_lds(u32x4& v) { asm volatile("s_waitcnt lgkmcnt(%1)" : "+v"(v) : "n"(N)); }
template <int N> __device__ __forceinline__ void wait_lds(u32x2& v) { asm volatile("s_waitcnt lgkmcnt(%1)" : "+v"(v) : "n"(N)); }
#define WIN 8
template <int T, int N> __device__ __forceinline__ void stair() {
  if constexpr (T == 0) __builtin_amdgcn_s_setprio(3);
  if constexpr (T == N * 3 / 8) __builtin_amdgcn_s_setprio(2);
  if constexpr (T == N * 5 / 8) __builtin_amdgcn_s_setprio(1);
  if constexpr (T == N * 7 / 8) __builtin_amdgcn_s_setprio(0);
}
template <int T0, int T1, int NT, int STR, bool FIRST, int JEND, int NA, int R, class WT>
__device__ __forceinline__ void k_steps(f32x4 (&acc)[NT], u32x4 (&a)[NA], u32x4 (&bf)[R], const unsigned (&lb)[3], WT wt) {
  if constexpr (FIRST) static_for<T0 * NT, T0 * NT + R - 1>([&](auto jc) { rd_b<jc.value, NT, STR>(bf[jc.value % R], lb); });
  static_for<T0 * NT, T1 * NT>([&](auto jc) {
    constexpr int j = jc.value, t = j / NT, n = j % NT;
    if constexpr (n == 0) wt(std::integral_constant<int, t>{});
    if constexpr (j + R - 1 < JEND) rd_b<j + R - 1, NT, STR>(bf[(j + R - 1) % R], lb);
    wait_lds<(j + R - 1 < JEND ? R - 1 : JEND - 1 - j)>(bf[j % R]);
    acc[n] = mma16(bf[j % R], a[t], acc[n]);
    SB();
  });
#pragma unroll
  for (int n = 0; n < NT; n++) asm volatile("" : "+v"(acc[n]));
}
template <bool WO>
__device__ __forceinline__ void epi80(Ctx& c, const Params& p, f32x4 (&acc)[5], const u32x4 (&e4)[2], const u32x2& e2, u32x2 (&hres)[5], int rb, int cb) {
  const int wm = c.w & 3, g = c.g, r16 = c.r16;
  LAS f32x4* red = (LAS f32x4*)c.lds + wm * 320 + c.lane;
  bar();
  if (c.w >= 4) {
#pragma unroll
    for (int n = 0; n < 5; n++) red[n * 64] = acc[n];
  }
  bar();
  if (c.w < 4) {
    const u32x2 ev[5] = {{e4[0][0], e4[0][1]}, {e4[0][2], e4[0][3]}, {e4[1][0], e4[1][1]}, {e4[1][2], e4[1][3]}, e2};
    u32x2 o[5];
#pragma unroll
    for (int n = 0; n < 5; n++) {
      f32x4 v = acc[n] + red[n * 64];
      v[0] += bflo(ev[n][0]); v[1] += bfhi(ev[n][0]); v[2] += bflo(ev[n][1]); v[3] += bfhi(ev[n][1]);
      if (!WO) { v[0] += bflo(hres[n][0]); v[1] += bfhi(hres[n][0]); v[2] += bflo(hres[n][1]); v[3] += bfhi(hres[n][1]); }
      o[n] = (u32x2){pk2(v[0], v[1]), pk2(v[2], v[3])};
    }
    const int row = rb * 64 + wm * 16 + r16;
    if (WO) {
      const int row16 = row >> 4;
#pragma unroll
      for (int q = 0; q < 2; q++) {
        const int c8 = cb * 10 + q * 4 + g;
        st16(c.rs, WS_H + ((row16 * 20 + (c8 >> 2)) * 64 + (c8 & 3) * 16 + r16) * 16, (u32x4){o[2 * q][0], o[2 * q][1], o[2 * q + 1][0], o[2 * q + 1][1]});
      }
      const int c8 = cb * 10 + 8 + (g >> 1);
      const auto x0 = __builtin_amdgcn_permlane16_swap(o[4][0], o[4][0], false, false);
      const auto x1 = __builtin_amdgcn_permlane16_swap(o[4][1], o[4][1], false, false);
      if (!(g & 1)) st16(c.rs, WS_H + ((row16 * 20 + (c8 >> 2)) * 64 + (c8 & 3) * 16 + r16) * 16, (u32x4){x0[0], x1[0], x0[1], x1[1]});
      float s1 = 0.f, s2 = 0.f;
#pragma unroll
      for (int n = 0; n < 5; n++) {
        hres[n] = o[n];
        const float a0 = bflo(o[n][0]), a1 = bfhi(o[n][0]), a2 = bflo(o[n][1]), a3 = bfhi(o[n][1]);
        s1 += (a0 + a1) + (a2 + a3);
        s2 += (a0 * a0 + a1 * a1) + (a2 * a2 + a3 * a3);
      }
      s1 += __shfl_xor(s1, 16); s2 += __shfl_xor(s2, 16);
      s1 += __shfl_xor(s1, 32); s2 += __shfl_xor(s2, 32);
      if (g == 0) st8(c.rs, WS_SP + row16 * 1024 + cb * 128 + r16 * 8, (u32x2){__builtin_bit_cast(unsigned, s1), __builtin_bit_cast(unsigned, s2)});
    } else {
      bf16* op = p.out + (size_t)row * D + cb * 80;
      *(u32x4*)(op + g * 8) = (u32x4){o[0][0], o[0][1], o[1][0], o[1][1]};
      *(u32x4*)(op + 32 + g * 8) = (u32x4){o[2][0], o[2][1], o[3][0], o[3][1]};
      *(u32x2*)(op + 64 + g * 4) = o[4];
    }
  }
}

__device__ __forceinline__ void tail_phases(Ctx& c, const Params& p, int xcd, int i, unsigned att_flag, u32x4 (&stash)[20]) {
  LAS char* lds = c.lds;
  const int lane = c.lane, w = c.w, r16 = c.r16, g = c.g;
  const int wm = w & 3, wk = w >> 2;
  const int rb64 = xcd * 4 + (i & 3), cb80 = i >> 2, rb128 = xcd * 2 + (i & 1), cb96 = i >> 1;
  const unsigned w1g = WS_W1 + cb96 * 122880, w2g = WS_W2 + cb80 * 245760, l16 = lane * 16;
  auto w2_fetch = [&](int slot) {
    const int ts = slot / 10, r = slot - ts * 10, h = r >= 5;
    blds16(c.rs, l16, w2g + ((h * 24 + (ts)) * 5 + r - 5 * h) * 1024, lds + slot * 1024);
  };
  const f32x4 z4 = {0.f, 0.f, 0.f, 0.f};
  u32x4 e4[2];
  u32x2 e2, hres[5];
  rawbar();
  if (w < 6) {
#pragma unroll
    for (int q = 0; q < 6; q++) blds16(c.rs, l16, w1g + ((w) * 6 + q) * 1024, lds + L_W1 + (w * 6 + q) * 1024);
  }
  {
    const bf16* xr = p.x + (size_t)(rb64 * 64 + wm * 16 + r16) * D + cb80 * 80;
    ld16(e4[0], xr + g * 8); ld16(e4[1], xr + 32 + g * 8); ld8(e2, xr + 64 + g * 4);
  }
  if (w < 6) wait_vm<9>(); else wait_vm<3>();
  rawbar();
  raise(c, att_flag);
  STAMP(7);
  await(c, F_ATT + xcd * 32 + lane, 32, 0, 0);
  STAMP(8);
  {
    u32x4 a[10], bf[11];
    const unsigned ag = WS_O + ((rb64 * 4 + wm) * 20 + wk * 10) * 1024;
#pragma unroll
    for (int t = 0; t < 10; t++) ld_frag(a[t], c, ag + (t) * 1024);
    const unsigned l0 = (unsigned)(size_t)lds + L_WO + wk * 51200 + lane * 16, lb[3] = {l0, l0, l0};
    f32x4 acc[5] = {z4, z4, z4, z4, z4};
    k_steps<0, 10, 5, 5120, true, 50>(acc, a, bf, lb, [&](auto tc) { wait_reg<9 - tc.value>(a[tc.value]); });
    STAMP(9);
    asm volatile("" : "+v"(e4[0]), "+v"(e4[1]), "+v"(e2));
    epi80<true>(c, p, acc, e4, e2, hres, rb64, cb80);
  }
  rawbar();
  if (w < 4) wait_vm<0>();
  else if (w < 7) {
#pragma unroll 1
    for (int t = 2 + w; t < 20; t += 3) {
#pragma unroll
      for (int q = 0; q < 6; q++) blds16(c.rs, l16, w1g + ((t) * 6 + q) * 1024, lds + L_W1 + (t * 6 + q) * 1024);
    }
  }
  rawbar();
  raise(c, F_WO + rb64 * 8 + cb80);
  STAMP(10);
  wait_vm<0>();
  rawbar();
  LAS float* st_cs = (LAS float*)(lds + L_CS);
  if (w < 6) {
    LAS char* bl = lds + L_W1 + lane * 16;
    const u32x4 ones = {0x3f803f80u, 0x3f803f80u, 0x3f803f80u, 0x3f803f80u};
    f32x4 cs0 = z4, cs1 = z4;
#pragma unroll
    for (int t = 0; t < 20; t += 2) {
      cs0 = mma16(ones, *(LAS u32x4*)(bl + (t * 6 + w) * 1024), cs0);
      cs1 = mma16(ones, *(LAS u32x4*)(bl + (t * 6 + 6 + w) * 1024), cs1);
    }
    if (g == 0) st_cs[w * 16 + r16] = cs0[0] + cs1[0];
    asm volatile("s_waitcnt lgkmcnt(0)" ::: "memory");
  }
  await(c, F_WO + rb128 * 16 + lane, 16, 0, 0);
  STAMP(11);
  {
    u32x4 a[20], bf[12], bv[3];
    u32x2 sp0, sp1;
    const int row16 = rb128 * 8 + w;
    {
      const char* q = c.ws + WS_SP + row16 * 1024 + g * 256 + r16 * 8;
      asm volatile("global_load_dwordx2 %0, %2, off sc1\n\tglobal_load_dwordx2 %1, %2, off offset:128 sc1" : "=&v"(sp0), "=&v"(sp1) : "v"(q));
    }
    const unsigned ag = WS_H + row16 * 20480;
#pragma unroll
    for (int t = 0; t < WIN; t++) ld_frag(a[t], c, ag + t * 1024);
#pragma unroll
    for (int j = 0; j < 3; j++) ld16(bv[j], p.b1 + cb96 * 96 + j * 32 + g * 8);
    LAS char* bl = lds + L_W1 + lane * 16;
    f32x4 acc[6] = {z4, z4, z4, z4, z4, z4};
    const unsigned l0 = (unsigned)(size_t)bl, lb[3] = {l0, l0 + 61440, l0};
    asm volatile("s_waitcnt lgkmcnt(0)" ::: "memory");
    k_steps<0, 20, 6, 6144, true, 120>(acc, a, bf, lb, [&](auto tc) {
      constexpr int t = tc.value;
      if constexpr (t >= 1 && t + WIN - 1 < 20) ld_frag(a[t + WIN - 1], c, ag + (t + WIN - 1) * 1024);
      if constexpr (t >= 14 && t < 18) { const int s = w * 4 + t - 14; w2_fetch(s < 20 ? s : (s < 30 ? s + 130 : 159)); }
      wait_reg<(t < WIN ? 3 : 0) + (t + WIN - 1 < 20 ? WIN - 1 : 19 - t) + (t >= 14 ? (t < 18 ? t - 13 : 4) : 0)>(a[t]);
      stair<t, 20>();
    });
    STAMP(12);
    asm volatile("" : "+v"(sp0), "+v"(sp1), "+v"(bv[0]), "+v"(bv[1]), "+v"(bv[2]));
    const f32x2 q2 = __builtin_bit_cast(f32x2, sp0) + __builtin_bit_cast(f32x2, sp1);
    float s1 = q2[0], s2 = q2[1];
    s1 += __shfl_xor(s1, 16); s2 += __shfl_xor(s2, 16);
    s1 += __shfl_xor(s1, 32); s2 += __shfl_xor(s2, 32);
    const float mean = s1 * (1.f / D);
    const float rs = __builtin_amdgcn_rsqf(fmaxf(s2 * (1.f / D) - mean * mean, 0.f) + 1e-5f);
#pragma unroll
    for (int j = 0; j < 3; j++) {
      u32x4 o;
#pragma unroll
      for (int h = 0; h < 2; h++) {
        const int n = 2 * j + h;
        const f32x4 c4 = *(LAS f32x4*)(st_cs + n * 16 + g * 4);
        const float v0 = fmaxf(rs * (acc[n][0] - mean * c4[0]) + bflo(bv[j][2 * h]), 0.f);
        const float v1 = fmaxf(rs * (acc[n][1] - mean * c4[1]) + bfhi(bv[j][2 * h]), 0.f);
        const float v2 = fmaxf(rs * (acc[n][2] - mean * c4[2]) + bflo(bv[j][2 * h + 1]), 0.f);
        const float v3 = fmaxf(rs * (acc[n][3] - mean * c4[3]) + bfhi(bv[j][2 * h + 1]), 0.f);
        o[2 * h] = pk2(v0 * v0, v1 * v1); o[2 * h + 1] = pk2(v2 * v2, v3 * v3);
      }
      st16(c.rs, WS_G + (row16 * 48 + cb96 * 3 + j) * 1024 + lane * 16, o);
    }
  }
  bar();
  if (w < 5) {
#pragma unroll
    for (int q = 0; q < 10; q++) w2_fetch(20 + w * 10 + q);
  }
  static_for<0, 10>([&](auto qc) { wr_b<qc.value * 1024>((unsigned)(size_t)lds + (70 + w * 10) * 1024 + lane * 16, stash[qc.value]); });
  {
    const bf16* br = p.b2 + cb80 * 80;
    ld16(e4[0], br + g * 8); ld16(e4[1], br + 32 + g * 8); ld8(e2, br + 64 + g * 4);
  }
  if (w < 5) wait_vm<13>(); else wait_vm<3>();
  bar();
  raise(c, F_FF1 + rb128 * 16 + cb96);
  STAMP(13);
  wait_vm<0>();
  await(c, F_FF1 + (rb64 >> 1) * 16 + lane, 16, 0, 0);
  STAMP(14);
  {
    u32x4 a[24], bf[11];
    const unsigned ag = WS_G + ((rb64 * 4 + wm) * 48 + wk * 24) * 1024;
#pragma unroll
    for (int t = 0; t < WIN; t++) ld_frag(a[t], c, ag + t * 1024);
    auto wt = [&](auto tc) {
      constexpr int t = tc.value;
      if constexpr (t >= 1 && t + WIN - 1 < 24) ld_frag(a[t + WIN - 1], c, ag + (t + WIN - 1) * 1024);
      wait_reg<(t + WIN - 1 < 24 ? WIN - 1 : 23 - t)>(a[t]);
      stair<t % 8, 8>();
    };
    const unsigned l0 = (unsigned)(size_t)lds + wk * 5120 + lane * 16, lb[3] = {l0, l0 + 61440, l0 + 122880};
    f32x4 acc[5] = {z4, z4, z4, z4, z4};
    k_steps<0, 8, 5, 10240, true, 80>(acc, a, bf, lb, wt);
    rawbar();
    asm volatile("s_waitcnt lgkmcnt(0)" ::: "memory");
    static_for<0, 10>([&](auto qc) { wr_b<qc.value * 1024>((unsigned)(size_t)lds + w * 10240 + lane * 16, stash[10 + qc.value]); });
    k_steps<8, 16, 5, 10240, false, 80>(acc, a, bf, lb, wt);
    rawbar();
    k_steps<16, 24, 5, 10240, true, 120>(acc, a, bf, lb, wt);
    STAMP(15);
    asm volatile("" : "+v"(e4[0]), "+v"(e4[1]), "+v"(e2));
    epi80<false>(c, p, acc, e4, e2, hres, rb64, cb80);
  }
}

__device__ __forceinline__ f32x16 mma32(u32x4 a, u32x4 b, f32x16 c) {
  return __builtin_amdgcn_mfma_f32_32x32x16_bf16(__builtin_bit_cast(bf16x8, a), __builtin_bit_cast(bf16x8, b), c, 0, 0, 0);
}
__device__ __forceinline__ f32x16 mma32h(u32x2 a, u32x2 b, f32x16 c) {
  return __builtin_amdgcn_mfma_f32_32x32x8bf16_1k(__builtin_bit_cast(s16x4, a), __builtin_bit_cast(s16x4, b), c, 0, 0, 0);
}
__device__ __forceinline__ float max16(const f32x16& s) {
  float m = fmaxf(fmaxf(s[0], s[1]), s[2]);
#pragma unroll
  for (int i = 3; i < 15; i += 2) m = fmaxf(fmaxf(m, s[i]), s[i + 1]);
  return fmaxf(m, s[15]);
}
__device__ __forceinline__ u32x2 set_shift(u32x2 qhi, float m, int hh) {
  const float a = bflo(pk2(m, 0.f));
  const float r = m - a;
  const float b = bflo(pk2(r, 0.f));
  const u32x2 e = {pk2(-a, -b), pk2(b - r, 0.f)};
  return hh ? e : qhi;
}
__device__ __forceinline__ u32x2 exp4(const f32x16& s, int i) {
  const float e0 = __builtin_amdgcn_exp2f(s[i]), e1 = __builtin_amdgcn_exp2f(s[i + 1]);
  const float e2 = __builtin_amdgcn_exp2f(s[i + 2]), e3 = __builtin_amdgcn_exp2f(s[i + 3]);
  return (u32x2){pk2(e0, e1), pk2(e2, e3)};
}

template <int OFF> __device__ __forceinline__ void rd8(u32x2& v, unsigned ad) { asm volatile("ds_read_b64 %0, %1 offset:%2" : "=v"(v) : "v"(ad), "n"(OFF)); }
template <int OFF> __device__ __forceinline__ void rd16(u32x4& v, unsigned ad) { asm volatile("ds_read_b128 %0, %1 offset:%2" : "=v"(v) : "v"(ad), "n"(OFF)); }
__device__ __forceinline__ unsigned local_s(unsigned v) { asm volatile("" : "+s"(v)); return v; }
__device__ __forceinline__ void wait_k(u32x2& a, u32x2& b, u32x2& h, u32x4& v) {
  asm volatile("s_waitcnt lgkmcnt(2)" : "+v"(a), "+v"(b), "+v"(h), "+v"(v));
}
template <class T> __device__ __forceinline__ void pin(T& v) { asm volatile("" : "+v"(v)); }
__device__ __forceinline__ void ld_flags(u32x4& a, u32x4& b, const char* fp) {
  asm volatile("global_load_dwordx4 %0, %2, off sc1\n\tglobal_load_dwordx4 %1, %2, off offset:16 sc1" : "=&v"(a), "=&v"(b) : "v"(fp));
}

constexpr int SCHED[] = {300, 0, 1, 106, 107, 2, 3, 301, 4, 5, 100, 101, 6, 7, 302, 8, 9, 102, 103, 311, 10, 11, 303, 12, 13, 104, 105, 310, 14, 15};

__device__ __forceinline__ void attn_phase(Ctx& c, const Params& p, int head, int qb, int i, u32x4 (&stash)[20]) {
  LAS char* lds = c.lds;
  const int lane = c.lane, w = c.w;
  const int r = lane & 31, hh = lane >> 5;
  const int qrow = qb * 256 + w * 32 + r;
  const unsigned kvg = WS_KV + head * KV_HEAD;
  const char* qp = c.ws + WS_Q + (size_t)(head * 5 * S + qrow) * 8;
  u32x2 qa, qc, qhi0;
  asm volatile("global_load_dwordx2 %0, %1, off sc1" : "=v"(qa) : "v"(qp + (size_t)(2 * hh) * S * 8));
  asm volatile("global_load_dwordx2 %0, %1, off sc1" : "=v"(qc) : "v"(qp + (size_t)(2 * hh + 1) * S * 8));
  asm volatile("global_load_dwordx2 %0, %1, off sc1" : "=v"(qhi0) : "v"(qp + (size_t)4 * S * 8));
  auto issue1 = [&](int s, int j) {
    blds16(c.rs, lane * 16, kvg + s * AT_STAGE + (w + 8 * j) * 1024, lds + (s % 3) * AT_STAGE + (w + 8 * j) * 1024);
  };
  const int job = blockIdx.x * 3 + w;
  LAS char* jl = lds + 2 * AT_STAGE + w * 6144;
  issue1(0, 0); issue1(0, 1);
  wait_vm<0>();
  asm volatile("" : "+v"(qa), "+v"(qc), "+v"(qhi0));
  const u32x4 qlo = {qa[0], qa[1], qc[0], qc[1]};
  bar();
  STAMP(5);
  issue1(0, 2); issue1(0, 3); issue1(0, 4); issue1(0, 5);
  if (w < 3) retile_load(c, p, job, jl);

  const int koff = hh * 512 + r * 8, khoff = 1024 + lane * 8, voff = 1536 + hh * 384 + (r <= 20 ? r : 21) * 16;
  const u32x2 qz = hh ? (u32x2){0u, 0u} : qhi0;
  const f32x16 zero = {0.f, 0.f, 0.f, 0.f, 0.f, 0.f, 0.f, 0.f, 0.f, 0.f, 0.f, 0.f, 0.f, 0.f, 0.f, 0.f};
  u32x2 qhi;
  f32x16 sc;
  {
    const u32x2 k0a = *(LAS u32x2*)(lds + koff), k0b = *(LAS u32x2*)(lds + koff + 256);
    const u32x4 k0 = {k0a[0], k0a[1], k0b[0], k0b[1]};
    const u32x2 kh0 = *(LAS u32x2*)(lds + khoff);
    const f32x16 s0 = mma32(k0, qlo, zero);
    float m = max16(mma32h(kh0, qz, s0));
    m = fmaxf(m, __shfl_xor(m, 32));
    qhi = set_shift(qhi0, m, hh);
    sc = mma32h(kh0, qhi, s0);
  }
  f32x16 o = zero;
  const unsigned l0 = (unsigned)(size_t)lds;
  u32x2 ka, kb, khi;
  u32x4 v0, v1;
  rd8<KV_TILE>(ka, l0 + koff); rd8<KV_TILE + 256>(kb, l0 + koff); rd8<KV_TILE>(khi, l0 + khoff);
  rd16<0>(v0, l0 + voff); rd16<768>(v1, l0 + voff);
  u32x4 p1p = {0u, 0u, 0u, 0u}, v1p = p1p, fr0 = p1p, fr1 = p1p;
  const int cb80 = i >> 2;
  unsigned ak[2], ah[2], av[2];
  float el[4] = {0.f, 0.f, 0.f, 0.f};
  auto slot = [&](auto tc, auto lc) {
    constexpr int t = tc.value;
    constexpr bool LAST = lc.value;
    constexpr int xk = t + 2 >= 16, ok = ((t + 2) & 15) * KV_TILE, xv = t + 1 >= 16, ov = ((t + 1) & 15) * KV_TILE;
    if constexpr (t == 14 && !LAST) __builtin_amdgcn_s_setprio(3);
    if constexpr (t == 4) __builtin_amdgcn_s_setprio(2);
    if constexpr (t == 9) __builtin_amdgcn_s_setprio(1);
    if constexpr (t == 12) __builtin_amdgcn_s_setprio(0);
    f32x16 sn;
    float e[16];
    unsigned pp[6];
    static_for<0, sizeof(SCHED) / sizeof(int)>([&](auto oc) {
      constexpr int op = SCHED[oc.value];
      if constexpr (op < 16) { e[op] = __builtin_amdgcn_exp2f(sc[op]); if constexpr (op >= 12) pin(e[op]); }
      else if constexpr (op < 106) { pp[op - 100] = pk2(e[2 * (op - 100)], e[2 * (op - 100) + 1]); pin(pp[op - 100]); }
      else if constexpr (op < 108) p1p[op - 104] = pk2(el[2 * (op - 106)], el[2 * (op - 106) + 1]);
      else if constexpr (op == 300) {
        sn = mma32((u32x4){ka[0], ka[1], kb[0], kb[1]}, qlo, zero);
      } else if constexpr (op == 301) {
        sn = mma32h(khi, qhi, sn);
        SB();
        rd8<ok>(ka, ak[xk]); rd8<ok + 256>(kb, ak[xk]); rd8<ok>(khi, ah[xk]);
      } else if constexpr (op == 302) {
        o = mma32(v1p, p1p, o);
      } else if constexpr (op == 310) {
        wait_k(ka, kb, khi, v1p);
      } else if constexpr (op == 311) {
        wait_lds<4>(v0);
      } else {
        o = mma32(v0, (u32x4){pp[0], pp[1], pp[2], pp[3]}, o);
        SB();
        v1p = v1;
        rd16<ov>(v0, av[xv]); rd16<ov + 768>(v1, av[xv]);
      }
      SB();
    });
    p1p[0] = pp[4]; p1p[1] = pp[5];
    el[0] = e[12]; el[1] = e[13]; el[2] = e[14]; el[3] = e[15];
    sc = sn;
  };
  auto warm = [&](unsigned voff, unsigned soff) {
    __builtin_amdgcn_raw_ptr_buffer_load_lds(c.rs, (LAS void*)(lds + 159744), 4, voff, soff, 0, 16);
  };
  const unsigned w1h = WS_W1 + (i >> 1) * 122880 + (i & 1) * 61440, w2p = WS_W2 + cb80 * 245760;
  const unsigned w2w = w2p + w * 5120;
  const unsigned w1v = w * 8192 + lane * 128 < 61440 ? w * 8192 + lane * 128 : 0;
  __builtin_amdgcn_s_setprio(3);
  wait_k(ka, kb, khi, v1p);
#pragma unroll 1
  for (int s = 0; s < 2; s++) {
    const unsigned cb = l0 + s * AT_STAGE, nb = cb + AT_STAGE;
    ak[0] = cb + koff; ak[1] = nb + koff; ah[0] = cb + khoff; ah[1] = nb + khoff; av[0] = cb + voff; av[1] = nb + voff;
    static_for<0, 16>([&](auto tc) {
      constexpr int t = tc.value;
      if constexpr (t < 6) { const unsigned o8 = local_s(s + 1) * AT_STAGE + (w + 8 * t) * 1024; blds16(c.rs, lane * 16, kvg + o8, lds + o8); }
      if constexpr (t == 3) { if (s == 0) { if (w < 3) wait_vm<9>(); else wait_vm<4>(); rawbar(); } }
      if constexpr (t == 9) { if (s == 0 && w < 3) { wait_vm<0>(); retile_store(c, p, local_s(job), jl); } }
      if constexpr (t == 9) { if (s == 1 && w == 7) ld_flags(fr0, fr1, c.ws + WS_FLAG + (F_RT + lane * 4) * 8); }
      if constexpr (t == 14) {
        CYC3(23);
        wait_vm<0>();
        if (s == 1 && w == 7) {
          wait_reg<0>(fr0); wait_reg<0>(fr1);
          const unsigned lo = (unsigned)c.token, hi = (unsigned)(c.token >> 32);
          if (!__all(fr0[0] == lo && fr0[1] == hi && fr0[2] == lo && fr0[3] == hi && fr1[0] == lo && fr1[1] == hi && fr1[2] == lo && fr1[3] == hi)) {
            spin_flags(c, F_RT + lane, 64, F_RT + 64 + lane, 64);
            spin_flags(c, F_RT + 128 + lane, 64, F_RT + 192 + lane, 64);
          }
        }
        rawbar();
        CYC3(24);
        if (s == 0) raise(c, F_RT + blockIdx.x);
      }
      slot(tc, std::false_type{});
    });
  }
  ak[0] = ak[1]; ah[0] = ah[1]; av[0] = av[1];
  ak[1] = l0 + koff; ah[1] = l0 + khoff; av[1] = l0 + voff;
  static_for<0, 16>([&](auto tc) {
    constexpr int t = tc.value;
    if constexpr (t < 6) issue1(3, t);
    else {
      constexpr int q0 = 2 * (t - 6), q1 = q0 + 1;
      ld_frag(stash[q0], c, w2w + (((q0 / 5 & 1) * 24 + (q0 < 10 ? 7 : 16)) * 5 + q0 % 5) * 1024);
      ld_frag(stash[q1], c, w2w + (((q1 / 5 & 1) * 24 + (q1 < 10 ? 7 : 16)) * 5 + q1 % 5) * 1024);
      if constexpr (t % 2 == 1 && t < 14) { if (w == (t - 7) / 2) warm(w1v, w1h); }
    }
    if constexpr (t == 14) { CYC(27); wait_vm<18>(); rawbar(); CYC(28); }
    SB();
    slot(tc, std::false_type{});
  });
  ak[0] = ak[1]; ah[0] = ah[1]; av[0] = av[1];
  static_for<0, 16>([&](auto tc) {
    constexpr int t = tc.value;
    if constexpr (t == 2) { CYC(29); wait_vm<0>(); rawbar(); CYC(30); }
    if constexpr (t >= 2 && t < 15) {
      if (t < 14 || w < 4) blds16(c.rs, lane * 16, WS_WO + cb80 * 102400 + (w + 8 * (t - 2)) * 1024, lds + L_WO + (w + 8 * (t - 2)) * 1024);
      if constexpr (t % 3 == 0) { if (w == 3 + t / 3) __builtin_amdgcn_raw_ptr_buffer_load_lds(c.rs, (LAS void*)(lds + AT_STAGE + w * 256), 4, w1v, w1h, 0, 16); }
      if constexpr (t % 3 == 1 && t < 13) { if (w == t / 3 - 1) __builtin_amdgcn_raw_ptr_buffer_load_lds(c.rs, (LAS void*)(lds + AT_STAGE + w * 256), 4, (w * 64 + lane < 140 ? w * 64 + lane : 0) * 128, w2p + (i & 1) * 122880 + ((i >> 1) & 1) * 17920, 0, 16); }
    }
    SB();
    slot(tc, std::true_type{});
  });
  __builtin_amdgcn_s_setprio(0);
  wait_lds<0>(v1p); wait_lds<0>(ka); wait_lds<0>(kb); wait_lds<0>(khi); wait_lds<0>(v0); wait_lds<0>(v1);
  p1p[2] = pk2(el[0], el[1]); p1p[3] = pk2(el[2], el[3]);
  o = mma32(v1p, p1p, o);
  STAMP(6);
  float l = __shfl(o[8], r + 32);
  if (!__all(l < 1.8446744e19f)) {
    const unsigned kb = WS_KV + head * KV_HEAD;
    auto gk = [&](int t) {
      const u32x2 a = __builtin_amdgcn_raw_buffer_load_b64(c.rs, kb + t * KV_TILE + koff, 0, 16);
      const u32x2 b = __builtin_amdgcn_raw_buffer_load_b64(c.rs, kb + t * KV_TILE + koff + 256, 0, 16);
      return mma32((u32x4){a[0], a[1], b[0], b[1]}, qlo, zero);
    };
    float m = -__builtin_inff();
#pragma unroll 1
    for (int t = 0; t < 64; t++)
      m = fmaxf(m, max16(mma32h(__builtin_amdgcn_raw_buffer_load_b64(c.rs, kb + t * KV_TILE + khoff, 0, 16), qz, gk(t))));
    m = fmaxf(m, __shfl_xor(m, 32));
    qhi = set_shift(qhi0, m, hh);
    o = zero;
#pragma unroll 1
    for (int t = 0; t < 64; t++) {
      const f32x16 s2 = mma32h(__builtin_amdgcn_raw_buffer_load_b64(c.rs, kb + t * KV_TILE + khoff, 0, 16), qhi, gk(t));
      const u32x2 pa = exp4(s2, 0), pb = exp4(s2, 4), pc = exp4(s2, 8), pd = exp4(s2, 12);
      o = mma32(__builtin_amdgcn_raw_buffer_load_b128(c.rs, kb + t * KV_TILE + voff, 0, 16), (u32x4){pa[0], pa[1], pb[0], pb[1]}, o);
      o = mma32(__builtin_amdgcn_raw_buffer_load_b128(c.rs, kb + t * KV_TILE + voff + 768, 0, 16), (u32x4){pc[0], pc[1], pd[0], pd[1]}, o);
    }
    l = __shfl(o[8], r + 32);
  }
  const float inv = 1.f / l;
  const auto x0 = __builtin_amdgcn_permlane32_swap(pk2(o[0] * inv, o[1] * inv), pk2(o[4] * inv, o[5] * inv), false, false);
  const auto x1 = __builtin_amdgcn_permlane32_swap(pk2(o[2] * inv, o[3] * inv), pk2(o[6] * inv, o[7] * inv), false, false);
  const int ca = head * DH + (head & 1) * 4 + hh * 8, cr = head * DH + ((head & 1) ? 0 : 16);
  const unsigned ob = WS_O + (qrow >> 4) * 20480 + (qrow & 15) * 16;
  st16(c.rs, ob + (ca >> 3) * 256, (u32x4){x0[0], x1[0], x0[1], x1[1]});
  if (hh == 0) st8(c.rs, ob + (cr >> 3) * 256 + (cr & 4) * 2, (u32x2){pk2(o[8] * inv, o[9] * inv), pk2(o[10] * inv, o[11] * inv)});
}

__global__ __launch_bounds__(512, 2) void k_block(Params p) {
  extern __shared__ __attribute__((aligned(16))) char smem[];
  Ctx c;
  c.tid = threadIdx.x; c.lane = c.tid & 63; c.w = __builtin_amdgcn_readfirstlane(c.tid >> 6);
  c.r16 = c.lane & 15; c.g = c.lane >> 4;
  c.lds = (LAS char*)smem; c.ws = p.ws;
  c.rs = __builtin_amdgcn_make_buffer_rsrc(p.ws, 0, WS_END, 0x00020000);
  c.rd = (u32x4){(unsigned)(size_t)p.ws, (unsigned)((size_t)p.ws >> 32) & 0xffffu, WS_END, 0x00020000u};
  STAMP(0);
  if (c.w == 7) {
    __builtin_amdgcn_raw_ptr_buffer_load_lds(c.rs, (LAS void*)(c.lds + 150016), 4, (c.lane * 6 + (blockIdx.x >> 3) % 6) * 65536, 0, 0, 0);
    const char* q = c.lane < 20 ? (const char*)p.out + c.lane * 131072 : c.lane < 27 ? (const char*)p.wo + (c.lane - 20) * 131072 : c.lane < 42 ? (const char*)p.w1 + (c.lane - 27) * 131072 : c.lane < 57 ? (const char*)p.w2 + (c.lane - 42) * 131072 : (const char*)p.b1;
    __builtin_amdgcn_global_load_lds((const GAS void*)q, (LAS void*)(c.lds + 150528), 4, 0, 0);
  }
  if (c.w == 6) {
    u64 pc;
    asm volatile("s_getpc_b64 %0" : "=s"(pc));
    const unsigned line = (blockIdx.x >> 3) + 32 * c.lane;
    if (line < 61000 / 128) __builtin_amdgcn_global_load_lds((const GAS void*)((pc & ~127ull) + line * 128), (LAS void*)(c.lds + 151040), 4, 0, 0);
  }
  const u64 epoch = __hip_atomic_load((const u64*)(p.ws + WS_FLAG) + F_EPOCH, __ATOMIC_RELAXED, __HIP_MEMORY_SCOPE_AGENT);
  const int b = blockIdx.x, xcd = b & 7, i = b >> 3;
  {
    const int rb = (xcd & 3) * 4 + (i & 3), cb = (xcd >> 2) * 8 + (i >> 2);
    if (cb < 10) qkv_phase<false>(c, p, rb, cb);
    else if (cb < 15) qkv_phase<true>(c, p, rb, cb);
    else if (c.w < 6) {
      retile_load(c, p, 768 + rb * 6 + c.w, c.lds + c.w * 6144);
      wait_vm<0>();
      retile_store(c, p, 768 + rb * 6 + c.w, c.lds + c.w * 6144);
    }
    u64 z = (epoch + 0x9e3779b97f4a7c15ull) * 0xbf58476d1ce4e5b9ull;
    c.token = z ^ (z >> 31);
    if (cb < 15) publish(c, F_QKV + cb * 16 + rb);
    STAMP(3);
  }
  const int head = xcd * 4 + (i & 3), qb = i >> 2;
  {
    const int c0 = head * 20 / 128, c1 = (head * 20 + 19) / 128, j = c.lane >> 4;
    await(c, F_QKV + (((j & 1) ? c1 : c0) + (j >> 1) * 5) * 16 + c.r16, 64, F_QKV + (((j & 1) ? c1 : c0) + 10) * 16 + c.r16, 32);
  }
  STAMP(4);
  u32x4 stash[20];
  attn_phase(c, p, head, qb, i, stash);
  tail_phases(c, p, xcd, i, F_ATT + qb * 32 + head, stash);
  if (b == 0 && c.tid == 0) __hip_atomic_store((u64*)(p.ws + WS_FLAG) + F_EPOCH, epoch + 1, __ATOMIC_RELAXED, __HIP_MEMORY_SCOPE_AGENT);
}

extern "C" void kernel_launch(void* const* d_in, const int* in_sizes, int n_in, void* d_out, int out_size, void* d_ws,
                              size_t ws_size, hipStream_t stream) {
  Params p;
  p.x = (const bf16*)d_in[0]; p.wqkv = (const bf16*)d_in[1]; p.wo = (const bf16*)d_in[2];
  p.w1 = (const bf16*)d_in[4]; p.b1 = (const bf16*)d_in[5]; p.w2 = (const bf16*)d_in[6]; p.b2 = (const bf16*)d_in[7];
  p.out = (bf16*)d_out; p.ws = (char*)d_ws;
  (void)hipFuncSetAttribute((const void*)k_block, hipFuncAttributeMaxDynamicSharedMemorySize, LDS_BYTES);
  k_block<<<256, 512, LDS_BYTES, stream>>>(p);
}
```

```cpp
#include <hip/hip_runtime.h>
#include <hip/hip_bf16.h>
#include <stdint.h>
#include <type_traits>

#define LAS __attribute__((address_space(3)))
#define GAS __attribute__((address_space(1)))
typedef __bf16 bf16;
typedef unsigned long long u64;
typedef __bf16 bf16x2 __attribute__((ext_vector_type(2)));
typedef __bf16 bf16x8 __attribute__((ext_vector_type(8)));
typedef short s16x4 __attribute__((ext_vector_type(4)));
typedef float f32x2 __attribute__((ext_vector_type(2)));
typedef float f32x4 __attribute__((ext_vector_type(4)));
typedef float f32x16 __attribute__((ext_vector_type(16)));
typedef unsigned u32x2 __attribute__((ext_vector_type(2)));
typedef unsigned u32x4 __attribute__((ext_vector_type(4)));
typedef __amdgpu_buffer_rsrc_t rsrc_t;

constexpr int S = 2048, D = 640, NH = 32, DH = 20, FF = 1536;
constexpr int KV_TILE = 3072;
constexpr int KV_HEAD = 64 * KV_TILE;
constexpr int AT_STAGE = 16 * KV_TILE;
constexpr int LDS_BYTES = 163840;
constexpr unsigned WS_FLAG = 0;
constexpr unsigned F_QKV = 0, F_ATT = 256, F_WO = 512, F_FF1 = 768, F_RT = 1024, F_EPOCH = 1536;
constexpr unsigned WS_Q = 16384;
constexpr unsigned WS_KV = WS_Q + NH * S * 40;
constexpr unsigned WS_O = WS_KV + NH * KV_HEAD;
constexpr unsigned WS_H = WS_O + S * D * 2;
constexpr unsigned WS_G = WS_H + S * D * 2;
constexpr unsigned WS_WO = WS_G + S * FF * 2;
constexpr unsigned WS_W1 = WS_WO + D * D * 2;
constexpr unsigned WS_W2 = WS_W1 + D * FF * 2;
constexpr unsigned WS_SP = WS_W2 + D * FF * 2;
constexpr unsigned WS_RS = WS_SP + S * 64;
constexpr unsigned WS_CP = WS_RS + S * 16;
constexpr unsigned WS_END = WS_CP + 15 * 10 * 1024;

#define SB() __builtin_amdgcn_sched_barrier(0)
template <int N> __device__ __forceinline__ void wait_vm() { asm volatile("s_waitcnt vmcnt(%0)" ::"n"(N) : "memory"); }
__device__ __forceinline__ void bar() { asm volatile("s_waitcnt lgkmcnt(0)" ::: "memory"); __builtin_amdgcn_s_barrier(); asm volatile("" ::: "memory"); }
__device__ __forceinline__ void rawbar() { asm volatile("" ::: "memory"); __builtin_amdgcn_s_barrier(); asm volatile("" ::: "memory"); }
template <int AUX> __device__ __forceinline__ void glds16(const void* g, LAS char* l) {
  __builtin_amdgcn_global_load_lds((const GAS void*)g, (LAS void*)l, 16, 0, AUX);
}
__device__ __forceinline__ void blds16(rsrc_t r, unsigned voff, unsigned soff, LAS char* l) {
  __builtin_amdgcn_raw_ptr_buffer_load_lds(r, (LAS void*)l, 16, voff, soff, 0, 16);
}
__device__ __forceinline__ void st16(rsrc_t r, unsigned off, u32x4 v) { __builtin_amdgcn_raw_buffer_store_b128(v, r, off, 0, 16); }
__device__ __forceinline__ void st8(rsrc_t r, unsigned off, u32x2 v) { __builtin_amdgcn_raw_buffer_store_b64(v, r, off, 0, 16); }
__device__ __forceinline__ unsigned pk2(float a, float b) {
  f32x2 f = {a, b};
  return __builtin_bit_cast(unsigned, __builtin_convertvector(f, bf16x2));
}
__device__ __forceinline__ float bflo(unsigned u) { return __builtin_bit_cast(float, u << 16); }
__device__ __forceinline__ float bfhi(unsigned u) { return __builtin_bit_cast(float, u & 0xffff0000u); }
__device__ __forceinline__ f32x4 mma16(u32x4 a, u32x4 b, f32x4 c) {
  return __builtin_amdgcn_mfma_f32_16x16x32_bf16(__builtin_bit_cast(bf16x8, a), __builtin_bit_cast(bf16x8, b), c, 0, 0, 0);
}
template <int I, int N, class F> __device__ __forceinline__ void static_for(F&& f) {
  if constexpr (I < N) { f(std::integral_constant<int, I>{}); static_for<I + 1, N>(f); }
}

struct Params {
  const bf16 *x, *wqkv, *wo, *w1, *b1, *w2, *b2;
  bf16* out;
  char* ws;
};
struct Ctx {
  int tid, lane, w, r16, g;
  LAS char* lds;
  char* ws;
  rsrc_t rs;
  u32x4 rd;
  u64 token, epoch;
};
#define CYC(i) do {} while (0)
#define CYC3(i) do {} while (0)
#define STAMP(i) do {} while (0)

__device__ __forceinline__ void publish(Ctx& c, unsigned flag) {
  wait_vm<0>();
  bar();
  if (c.tid == 0) __hip_atomic_store((u64*)(c.ws + WS_FLAG) + flag, c.token, __ATOMIC_RELAXED, __HIP_MEMORY_SCOPE_AGENT);
}
__device__ __forceinline__ void spin_flags(Ctx& c, unsigned idx0, int n0, unsigned idx1, int n1) {
  const u64* f = (const u64*)(c.ws + WS_FLAG);
  for (int it = 0; it < (1 << 20); it++) {
    u64 a = c.token, b = c.token;
    if (c.lane < n0) a = __hip_atomic_load(f + idx0, __ATOMIC_RELAXED, __HIP_MEMORY_SCOPE_AGENT);
    if (c.lane < n1) b = __hip_atomic_load(f + idx1, __ATOMIC_RELAXED, __HIP_MEMORY_SCOPE_AGENT);
    if (__all(a == c.token && b == c.token)) break;
    __builtin_amdgcn_s_sleep(1);
  }
}
__device__ __forceinline__ void await(Ctx& c, unsigned idx0, int n0, unsigned idx1, int n1) {
  if (c.w == 7) spin_flags(c, idx0, n0, idx1, n1);
  rawbar();
}

struct RowLd { u32x4 a; unsigned b; u64 ep; };
__device__ __forceinline__ void row_load(RowLd& r, const Ctx& c, const Params& p, int row) {
  const char* x = (const char*)(p.x + (size_t)row * D);
  asm volatile("global_load_dwordx2 %0, %3, off sc1\n\tglobal_load_dwordx4 %1, %4, off\n\tglobal_load_dword %2, %5, off"
               : "=&v"(r.ep), "=&v"(r.a), "=&v"(r.b) : "v"(p.ws + WS_FLAG + F_EPOCH * 8), "v"(x + c.lane * 16), "v"(x + 1024 + c.lane * 4));
}
__device__ __forceinline__ void row_landed(RowLd& r, Ctx& c) {
  asm volatile("" : "+v"(r.ep), "+v"(r.a), "+v"(r.b));
  c.epoch = ((u64)__builtin_amdgcn_readfirstlane((unsigned)(r.ep >> 32)) << 32) | __builtin_amdgcn_readfirstlane((unsigned)r.ep);
  const u64 z = (c.epoch + 0x9e3779b97f4a7c15ull) * 0xbf58476d1ce4e5b9ull;
  c.token = z ^ (z >> 31);
}
__device__ __forceinline__ float wave_sum(float v) {
  v += __builtin_bit_cast(float, __builtin_amdgcn_update_dpp(0, __builtin_bit_cast(int, v), 0x128, 0xf, 0xf, false));
  v += __builtin_bit_cast(float, __builtin_amdgcn_update_dpp(0, __builtin_bit_cast(int, v), 0x124, 0xf, 0xf, false));
  v += __builtin_bit_cast(float, __builtin_amdgcn_update_dpp(0, __builtin_bit_cast(int, v), 0x122, 0xf, 0xf, false));
  v += __builtin_bit_cast(float, __builtin_amdgcn_update_dpp(0, __builtin_bit_cast(int, v), 0x121, 0xf, 0xf, false));
  return v;
}
__device__ __forceinline__ float rows_sum(float v) {
  const unsigned u = __builtin_bit_cast(unsigned, v);
  const auto s = __builtin_amdgcn_permlane16_swap(u, u, false, false);
  const unsigned s0 = s[0], s1 = s[1];
  const unsigned m = __builtin_bit_cast(unsigned, __builtin_bit_cast(float, s0) + __builtin_bit_cast(float, s1));
  const auto q = __builtin_amdgcn_permlane32_swap(m, m, false, false);
  const unsigned q0 = q[0], q1 = q[1];
  return __builtin_bit_cast(float, q0) + __builtin_bit_cast(float, q1);
}
__device__ __forceinline__ void row_stats(const RowLd& r, const Ctx& c, int row) {
  float s1 = bflo(r.b) + bfhi(r.b), s2 = bflo(r.b) * bflo(r.b) + bfhi(r.b) * bfhi(r.b);
#pragma unroll
  for (int j = 0; j < 4; j++) { const float lo = bflo(r.a[j]), hi = bfhi(r.a[j]); s1 += lo + hi; s2 += lo * lo + hi * hi; }
  const float mean = rows_sum(wave_sum(s1)) * (1.f / D);
  const float rstd = __builtin_amdgcn_rsqf(fmaxf(rows_sum(wave_sum(s2)) * (1.f / D) - mean * mean, 0.f) + 1e-5f);
  const unsigned tag = (unsigned)c.token;
  if (c.lane == 0) st16(c.rs, WS_RS + row * 16, (u32x4){__builtin_bit_cast(unsigned, mean), tag, __builtin_bit_cast(unsigned, rstd), tag});
}

__device__ __forceinline__ void gather(u32x4& s, u32x2 (&q)[3], const char* sp, const char* cp, int o2) {
  asm volatile("global_load_dwordx4 %0, %4, off sc1\n\tglobal_load_dwordx2 %1, %5, off sc1\n\tglobal_load_dwordx2 %2, %6, off sc1\n\tglobal_load_dwordx2 %3, %7, off sc1"
               : "=&v"(s), "=&v"(q[0]), "=&v"(q[1]), "=&v"(q[2]) : "v"(sp), "v"(cp), "v"(cp + 4096), "v"(cp + o2));
}
template <bool ISV>
__device__ __forceinline__ void qkv_phase(Ctx& c, const Params& p, int rb, int cb) {
  constexpr int NS = 4, NKT = 10, STAGE = 32768, LDB = 3 * D;
  LAS char* lds = c.lds;
  LAS float* st_mean = (LAS float*)(lds + NS * STAGE);
  LAS float* st_rstd = st_mean + 128;
  LAS float* st_cs = st_rstd + 128;
  const int lane = c.lane, w = c.w, r16 = c.r16, g = c.g;
  const int wm = w & 3, wn = w >> 2;
  const int row0 = rb * 128, col0 = cb * 128;
  const int rot = (rb + cb) % NKT;

  const char* srcA[2];
  const char* srcB[2];
#pragma unroll
  for (int i = 0; i < 2; i++) {
    const int pc = w + 8 * i;
    {
      const int lr = pc * 8 + (lane >> 3), rho = lr & 31;
      const int gr = ISV ? (16 * ((rho >> 3) & 1) + 8 * (rho >> 4) + (rho & 7)) : (2 * (rho & 15) + (rho >> 4));
      const int ch = (lane & 7) ^ ((lr >> 1) & 7);
      srcA[i] = (const char*)(p.x + (size_t)(row0 + (lr & ~31) + gr) * D + ch * 8);
    }
    {
      const int row = lane >> 3, f = ((row >> 1) & 1) | ((w & 1) << 1);
      const int ch = (lane & 7) ^ (f << 1);
      srcB[i] = (const char*)(p.wqkv + (size_t)(w * 8 + row) * LDB + col0 + i * 64 + ch * 8);
    }
  }
  auto issue = [&](int t, int i) {
    int kt = t + rot;
    if (kt >= NKT) kt -= NKT;
    LAS char* sb = lds + (t % NS) * STAGE;
    if (i < 2) glds16<0>(srcA[i] + kt * 128, sb + (w + 8 * i) * 1024);
    else glds16<0>(srcB[i - 2] + (size_t)kt * (64 * LDB * 2), sb + 16384 + (w + 8 * (i - 2)) * 1024);
  };
  RowLd rl;
  row_load(rl, c, p, row0 + cb * 8 + w);
#pragma unroll
  for (int i = 0; i < 4; i++) issue(0, i);

  int aoff[2], boff[4];
#pragma unroll
  for (int m = 0; m < 2; m++) {
    const int row = wm * 32 + (m ^ wn) * 16 + r16;
    aoff[m] = row * 128 + ((g ^ ((row >> 1) & 7)) << 4);
  }
#pragma unroll
  for (int n = 0; n < 4; n++) {
    const int q = (lane >> 2) & 3, pp = lane & 3, f = ((q >> 1) & 1) | ((g & 1) << 1);
    const int nt = (n + wm) & 3;
    boff[n] = 16384 + wn * 8192 + (g * 8 + q) * 128 + (((nt * 2 + (pp >> 1)) ^ (f << 1)) << 4) + (pp & 1) * 8;
  }
  f32x4 acc[2][4], a_cs = {0.f, 0.f, 0.f, 0.f};
  u32x4 gs;
  u32x2 gc[3];
  const int rho = (w & 1) * 16 + r16;
  const char* gsp = c.ws + WS_RS + (row0 + (w >> 1) * 32 + (ISV ? 16 * ((rho >> 3) & 1) + 8 * (rho >> 4) + (rho & 7) : 2 * r16 + (w & 1))) * 16;
  const char* gcp = c.ws + WS_CP + (cb * 10 + g) * 1024 + (w * 16 + r16) * 8;
  const int g2 = g < 2 ? 8192 : 0;
#pragma unroll
  for (int m = 0; m < 2; m++)
#pragma unroll
    for (int n = 0; n < 4; n++) acc[m][n] = a_cs;
  const u32x4 ones = {0x3f803f80u, 0x3f803f80u, 0x3f803f80u, 0x3f803f80u};
  u32x4 af[2][2][2];
  u32x2 bq[2][2][4][2];
  auto read1 = [&](int t, int r) {
    LAS char* sb = lds + (t % NS) * STAGE;
    const int bi = t & 1;
    if (r < 8) {
      const int kk = r >> 2, n = r & 3;
      const unsigned ad = (unsigned)(size_t)(sb + boff[n]);
      if (kk == 0) asm volatile("ds_read_b64_tr_b16 %0, %2\n\tds_read_b64_tr_b16 %1, %2 offset:512"
                                : "=&v"(bq[bi][0][n][0]), "=&v"(bq[bi][0][n][1]) : "v"(ad));
      else asm volatile("ds_read_b64_tr_b16 %0, %2 offset:4096\n\tds_read_b64_tr_b16 %1, %2 offset:4608"
                        : "=&v"(bq[bi][1][n][0]), "=&v"(bq[bi][1][n][1]) : "v"(ad));
    } else {
      const int kk = (r - 8) >> 1, m = r & 1;
      af[bi][kk][m] = *(LAS u32x4*)(sb + (aoff[m] ^ (kk << 6)));
    }
  };
#pragma unroll
  for (int i = 0; i < 4; i++) issue(1, i);
  wait_vm<4>();
  row_landed(rl, c);
  bar();
  STAMP(1);
#pragma unroll
  for (int r = 0; r < 12; r++) { read1(0, r); if (r < 8) issue(2 + (r >> 2), r & 3); }
  static_for<0, NKT>([&](auto tc) {
    constexpr int t = tc.value, bi = t & 1;
    if constexpr (t + 1 < NKT) wait_vm<4 * ((NKT - 2 - t) < (NS - 2) ? (NKT - 2 - t) : (NS - 2))>();
    asm volatile("s_waitcnt lgkmcnt(0)"
                 : "+v"(bq[bi][0][0][0]), "+v"(bq[bi][0][0][1]), "+v"(bq[bi][1][0][0]), "+v"(bq[bi][1][0][1]),
                   "+v"(bq[bi][0][1][0]), "+v"(bq[bi][0][1][1]), "+v"(bq[bi][1][1][0]), "+v"(bq[bi][1][1][1]),
                   "+v"(bq[bi][0][2][0]), "+v"(bq[bi][0][2][1]), "+v"(bq[bi][1][2][0]), "+v"(bq[bi][1][2][1]),
                   "+v"(bq[bi][0][3][0]), "+v"(bq[bi][0][3][1]), "+v"(bq[bi][1][3][0]), "+v"(bq[bi][1][3][1]));
    if constexpr (t + 1 < NKT) bar();
    SB();
#pragma unroll
    for (int i = 0; i < 16; i++) {
      const int kk = i >> 3, m = (i >> 2) & 1, n = i & 3;
      const u32x4 bv = {bq[bi][kk][n][0][0], bq[bi][kk][n][0][1], bq[bi][kk][n][1][0], bq[bi][kk][n][1][1]};
      acc[m][n] = ISV ? mma16(af[bi][kk][m], bv, acc[m][n]) : mma16(bv, af[bi][kk][m], acc[m][n]);
      if (t == 0 && (i & 7) == 0) a_cs = mma16(ones, bv, a_cs);
      if (t + 1 < NKT && i < 12) read1(t + 1, i < 4 ? i : (i < 6 ? i + 4 : (i < 10 ? i - 2 : i)));
      if (t + NS < NKT && (i & 3) == 2) issue(t + NS, i >> 2);
      if (t == 0 && i == 13) row_stats(rl, c, row0 + cb * 8 + w);
      if (t == 1 && i == 13 && rb < 10 && g == 0)
        st8(c.rs, WS_CP + (cb * 10 + rot) * 1024 + ((wn * 4 + wm) * 16 + r16) * 8, (u32x2){__builtin_bit_cast(unsigned, a_cs[0] + 0.f), (unsigned)c.token});
      if (t == 8 && i == 1) gather(gs, gc, gsp, gcp, g2);
      SB();
    }
  });
  STAMP(2);
  {
    const unsigned tag = (unsigned)c.token;
    for (int it = 0; it < (1 << 20); it++) {
      asm volatile("s_waitcnt vmcnt(0)" : "+v"(gs), "+v"(gc[0]), "+v"(gc[1]), "+v"(gc[2]));
      if (__all(gs[1] == tag && gs[3] == tag && gc[0][1] == tag && gc[1][1] == tag && gc[2][1] == tag)) break;
      gather(gs, gc, gsp, gcp, g2);
    }
    const unsigned c0 = gc[0][0], c1 = gc[1][0], c2 = gc[2][0], m0 = gs[0], r0 = gs[2];
    const float cs = rows_sum(__builtin_bit_cast(float, c0) + __builtin_bit_cast(float, c1) + (g < 2 ? __builtin_bit_cast(float, c2) : 0.f));
    if (g == 0) {
      st_mean[w * 16 + r16] = __builtin_bit_cast(float, m0);
      st_rstd[w * 16 + r16] = __builtin_bit_cast(float, r0);
      st_cs[w * 16 + r16] = cs;
    }
    bar();
  }
  const int tile = (row0 >> 5) + wm;
  if (!ISV) {
    const bool isq = cb < 5;
    float mean[2], rs[2];
#pragma unroll
    for (int m = 0; m < 2; m++) {
      mean[m] = st_mean[wm * 32 + (m ^ wn) * 16 + r16];
      rs[m] = st_rstd[wm * 32 + (m ^ wn) * 16 + r16] * (isq ? 0.22360679774997896f * 1.4426950408889634f : 1.f);
    }
#pragma unroll
    for (int n = 0; n < 4; n++) {
      const int lc = (wn * 4 + ((n + wm) & 3)) * 16 + g * 4;
      const f32x4 c4 = *(LAS f32x4*)(st_cs + lc);
      u32x2 o[2];
#pragma unroll
      for (int m = 0; m < 2; m++) {
        o[m][0] = pk2(rs[m] * (acc[m][n][0] - mean[m] * c4[0]), rs[m] * (acc[m][n][1] - mean[m] * c4[1]));
        o[m][1] = pk2(rs[m] * (acc[m][n][2] - mean[m] * c4[2]), rs[m] * (acc[m][n][3] - mean[m] * c4[3]));
      }
      const u32x4 v = wn ? (u32x4){o[1][0], o[1][1], o[0][0], o[0][1]} : (u32x4){o[0][0], o[0][1], o[1][0], o[1][1]};
      const unsigned cc = col0 + lc - (isq ? 0 : D);
      if (isq) {
        st16(c.rs, WS_Q + ((cc >> 2) * S + tile * 32 + 2 * r16) * 8, v);
      } else {
        const unsigned head = cc / 20u, d0 = cc - head * 20u;
        const unsigned t = WS_KV + head * KV_HEAD + tile * KV_TILE + r16 * 16;
        st16(c.rs, t + d0 * 64, v);
        if (d0 == 16) st16(c.rs, t + 1280, (u32x4){0x3f803f80u, 0x00003f80u, 0x3f803f80u, 0x00003f80u});
      }
    }
  } else {
    f32x4 mean[2], rs[2];
#pragma unroll
    for (int m = 0; m < 2; m++) {
      mean[m] = *(LAS f32x4*)(st_mean + wm * 32 + (m ^ wn) * 16 + g * 4);
      rs[m] = *(LAS f32x4*)(st_rstd + wm * 32 + (m ^ wn) * 16 + g * 4);
    }
#pragma unroll
    for (int n = 0; n < 4; n++) {
      const int lc = (wn * 4 + ((n + wm) & 3)) * 16 + r16;
      const float cs = st_cs[lc];
      u32x2 o[2];
#pragma unroll
      for (int m = 0; m < 2; m++) {
        o[m][0] = pk2(rs[m][0] * (acc[m][n][0] - mean[m][0] * cs), rs[m][1] * (acc[m][n][1] - mean[m][1] * cs));
        o[m][1] = pk2(rs[m][2] * (acc[m][n][2] - mean[m][2] * cs), rs[m][3] * (acc[m][n][3] - mean[m][3] * cs));
      }
      const u32x4 v = wn ? (u32x4){o[1][0], o[1][1], o[0][0], o[0][1]} : (u32x4){o[0][0], o[0][1], o[1][0], o[1][1]};
      const unsigned cc = col0 + lc - 2 * D;
      const unsigned head = cc / 20u, d = cc - head * 20u;
      const unsigned t = WS_KV + tile * KV_TILE + 1536 + g * 384, odd = head & 1;
      const unsigned row = odd ? (d < 4 ? d + 16 : d - 4) : d, e = (row & 3) ? 0u : 0x3f803f80u;
      const bool mix = row >= 16;
      const u32x4 cv = {e, e, e, e};
      st16(c.rs, t + (mix ? head & ~1u : head) * KV_HEAD + (mix && odd ? row + 4 : row) * 16, mix && odd ? cv : v);
      if (mix) st16(c.rs, t + (head | 1u) * KV_HEAD + (odd ? row : row + 4) * 16, odd ? v : cv);
    }
  }
}

__device__ __forceinline__ void retile_job(const Params& p, int j, const bf16*& W, unsigned& dst, int& cpr) {
  if (j < 160) { const int ks = j >> 3, blk = j & 7; W = p.wo + ks * 32 * D + blk * 80; dst = WS_WO + (blk * 20 + ks) * 5120; cpr = 10; }
  else if (j < 480) { const int e = j - 160, ks = e >> 4, blk = e & 15; W = p.w1 + ks * 32 * FF + blk * 96; dst = WS_W1 + (blk * 20 + ks) * 6144; cpr = 12; }
  else { const int e = j - 480, ks = e >> 3, blk = e & 7; W = p.w2 + ks * 32 * D + blk * 80; dst = WS_W2 + (blk * 48 + ks) * 5120; cpr = 10; }
}
__device__ __forceinline__ void retile_load(Ctx& c, const Params& p, int j, LAS char* base) {
  const bf16* W; unsigned dst; int cpr;
  retile_job(p, j, W, dst, cpr);
  const int ld = cpr == 12 ? FF : D;
#pragma unroll
  for (int i = 0; i < 6; i++) {
    const int q = i * 64 + c.lane, row = cpr == 12 ? q / 12 : q / 10, ch = q - row * cpr;
    if (i < 5 || cpr == 12) glds16<0>(W + row * ld + ch * 8, base + i * 1024);
  }
}
__device__ __forceinline__ void retile_store(Ctx& c, const Params& p, int j, LAS char* base) {
  const bf16* W; unsigned dst; int cpr;
  retile_job(p, j, W, dst, cpr);
  const int st = cpr * 16, pp = c.lane & 3;
  const unsigned ad = (unsigned)(size_t)base + (c.g * 8 + ((c.lane >> 2) & 3)) * st;
#pragma unroll
  for (int n = 0; n < 6; n++) {
    if (n < 5 || cpr == 12) {
      const int cq = (n >= 4 && cpr == 10) ? 16 + pp : 8 * (n >> 1) + 2 * pp + (n & 1);
      u32x2 r0, r1;
      asm volatile("ds_read_b64_tr_b16 %0, %2\n\tds_read_b64_tr_b16 %1, %3\n\ts_waitcnt lgkmcnt(0)"
                   : "=&v"(r0), "=&v"(r1) : "v"(ad + cq * 8), "v"(ad + cq * 8 + 4 * st) : "memory");
      st16(c.rs, dst + n * 1024 + c.lane * 16, (u32x4){r0[0], r0[1], r1[0], r1[1]});
    }
  }
}

constexpr int L_WO = 61440, L_W1 = 20480, L_CS = 143360;
__device__ __forceinline__ void ld16s(u32x4& v, const char* p) { asm volatile("global_load_dwordx4 %0, %1, off sc1" : "=v"(v) : "v"(p)); }
__device__ __forceinline__ void ld_frag(u32x4& v, const Ctx& c, unsigned soff) {
  asm volatile("buffer_load_dwordx4 %0, %1, %2, %3 offen sc1" : "=v"(v) : "v"(c.lane * 16), "s"(c.rd), "s"(soff));
}
__device__ __forceinline__ void ld16(u32x4& v, const void* p) { asm volatile("global_load_dwordx4 %0, %1, off" : "=v"(v) : "v"(p)); }
__device__ __forceinline__ void ld8(u32x2& v, const void* p) { asm volatile("global_load_dwordx2 %0, %1, off" : "=v"(v) : "v"(p)); }
template <int N> __device__ __forceinline__ void wait_reg(u32x4& v) { asm volatile("s_waitcnt vmcnt(%1)" : "+v"(v) : "n"(N)); }
__device__ __forceinline__ void raise(Ctx& c, unsigned flag) {
  if (c.tid == 0) __hip_atomic_store((u64*)(c.ws + WS_FLAG) + flag, c.token, __ATOMIC_RELAXED, __HIP_MEMORY_SCOPE_AGENT);
}
template <int J, int NT, int STR> __device__ __forceinline__ void rd_b(u32x4& v, const unsigned (&lb)[3]) {
  constexpr int t = J / NT, tt = STR == 10240 ? (t & 15) : t, per = 61440 / STR;
  asm volatile("ds_read_b128 %0, %1 offset:%2" : "=v"(v) : "v"(lb[tt / per]), "n"((tt % per) * STR + (J % NT) * 1024));
}
template <int OFF> __device__ __forceinline__ void wr_b(unsigned ad, const u32x4& v) {
  asm volatile("ds_write_b128 %0, %1 offset:%2" :: "v"(ad), "v"(v), "n"(OFF) : "memory");
}
template <int N> __device__ __forceinline__ void wait_lds(u32x4& v) { asm volatile("s_waitcnt lgkmcnt(%1)" : "+v"(v) : "n"(N)); }
template <int N> __device__ __forceinline__ void wait_lds2(u32x4& v, u32x4& u) { asm volatile("s_waitcnt lgkmcnt(%2)" : "+v"(v), "+v"(u) : "n"(N)); }
template <int N> __device__ __forceinline__ void wait_lds(u32x2& v) { asm volatile("s_waitcnt lgkmcnt(%1)" : "+v"(v) : "n"(N)); }
#define WIN 8
template <int T, int N> __device__ __forceinline__ void stair() {
  if constexpr (T == 0) __builtin_amdgcn_s_setprio(3);
  if constexpr (T == N * 3 / 8) __builtin_amdgcn_s_setprio(2);
  if constexpr (T == N * 5 / 8) __builtin_amdgcn_s_setprio(1);
  if constexpr (T == N * 7 / 8) __builtin_amdgcn_s_setprio(0);
}
template <int T0, int T1, int NT, int STR, bool FIRST, int JEND, int NA, int R, class WT>
__device__ __forceinline__ void k_steps(f32x4 (&acc)[NT], u32x4 (&a)[NA], u32x4 (&bf)[R], const unsigned (&lb)[3], WT wt) {
  if constexpr (FIRST) static_for<T0 * NT, T0 * NT + R - 1>([&](auto jc) { rd_b<jc.value, NT, STR>(bf[jc.value % R], lb); });
  wait_lds<R - 2>(bf[T0 * NT % R]);
  static_for<T0 * NT, T1 * NT>([&](auto jc) {
    constexpr int j = jc.value, t = j / NT, n = j % NT, last = (j + R - 1 < JEND ? j + R - 1 : JEND - 1);
    if constexpr (n == 0) wt(std::integral_constant<int, t>{});
    if constexpr (j + R - 1 < JEND) rd_b<j + R - 1, NT, STR>(bf[(j + R - 1) % R], lb);
    if constexpr ((j - T0 * NT) % 2 == 0 && j + 1 < T1 * NT) {
      if constexpr (j + 2 < T1 * NT) wait_lds2<last - j - 2>(bf[(j + 1) % R], bf[(j + 2) % R]);
      else wait_lds<last - j - 1>(bf[(j + 1) % R]);
    }
    acc[n] = mma16(bf[j % R], a[t], acc[n]);
    SB();
  });
#pragma unroll
  for (int n = 0; n < NT; n++) asm volatile("" : "+v"(acc[n]));
}
template <bool WO>
__device__ __forceinline__ void epi80(Ctx& c, const Params& p, f32x4 (&acc)[5], const u32x4 (&e4)[2], const u32x2& e2, u32x2 (&hres)[5], int rb, int cb) {
  const int wm = c.w & 3, g = c.g, r16 = c.r16;
  LAS f32x4* red = (LAS f32x4*)c.lds + wm * 320 + c.lane;
  bar();
  if (c.w >= 4) {
#pragma unroll
    for (int n = 0; n < 5; n++) red[n * 64] = acc[n];
  }
  bar();
  if (c.w < 4) {
    const u32x2 ev[5] = {{e4[0][0], e4[0][1]}, {e4[0][2], e4[0][3]}, {e4[1][0], e4[1][1]}, {e4[1][2], e4[1][3]}, e2};
    u32x2 o[5];
#pragma unroll
    for (int n = 0; n < 5; n++) {
      f32x4 v = acc[n] + red[n * 64];
      v[0] += bflo(ev[n][0]); v[1] += bfhi(ev[n][0]); v[2] += bflo(ev[n][1]); v[3] += bfhi(ev[n][1]);
      if (!WO) { v[0] += bflo(hres[n][0]); v[1] += bfhi(hres[n][0]); v[2] += bflo(hres[n][1]); v[3] += bfhi(hres[n][1]); }
      o[n] = (u32x2){pk2(v[0], v[1]), pk2(v[2], v[3])};
    }
    const int row = rb * 64 + wm * 16 + r16;
    if (WO) {
      const int row16 = row >> 4;
#pragma unroll
      for (int q = 0; q < 2; q++) {
        const int c8 = cb * 10 + q * 4 + g;
        st16(c.rs, WS_H + ((row16 * 20 + (c8 >> 2)) * 64 + (c8 & 3) * 16 + r16) * 16, (u32x4){o[2 * q][0], o[2 * q][1], o[2 * q + 1][0], o[2 * q + 1][1]});
      }
      const int c8 = cb * 10 + 8 + (g >> 1);
      const auto x0 = __builtin_amdgcn_permlane16_swap(o[4][0], o[4][0], false, false);
      const auto x1 = __builtin_amdgcn_permlane16_swap(o[4][1], o[4][1], false, false);
      if (!(g & 1)) st16(c.rs, WS_H + ((row16 * 20 + (c8 >> 2)) * 64 + (c8 & 3) * 16 + r16) * 16, (u32x4){x0[0], x1[0], x0[1], x1[1]});
      float s1 = 0.f, s2 = 0.f;
#pragma unroll
      for (int n = 0; n < 5; n++) {
        hres[n] = o[n];
        const float a0 = bflo(o[n][0]), a1 = bfhi(o[n][0]), a2 = bflo(o[n][1]), a3 = bfhi(o[n][1]);
        s1 += (a0 + a1) + (a2 + a3);
        s2 += (a0 * a0 + a1 * a1) + (a2 * a2 + a3 * a3);
      }
      s1 += __shfl_xor(s1, 16); s2 += __shfl_xor(s2, 16);
      s1 += __shfl_xor(s1, 32); s2 += __shfl_xor(s2, 32);
      if (g == 0) st8(c.rs, WS_SP + row16 * 1024 + cb * 128 + r16 * 8, (u32x2){__builtin_bit_cast(unsigned, s1), __builtin_bit_cast(unsigned, s2)});
    } else {
      bf16* op = p.out + (size_t)row * D + cb * 80;
      *(u32x4*)(op + g * 8) = (u32x4){o[0][0], o[0][1], o[1][0], o[1][1]};
      *(u32x4*)(op + 32 + g * 8) = (u32x4){o[2][0], o[2][1], o[3][0], o[3][1]};
      *(u32x2*)(op + 64 + g * 4) = o[4];
    }
  }
}

__device__ __forceinline__ void tail_phases(Ctx& c, const Params& p, int xcd, int i, unsigned att_flag, u32x4 (&stash)[20]) {
  LAS char* lds = c.lds;
  const int lane = c.lane, w = c.w, r16 = c.r16, g = c.g;
  const int wm = w & 3, wk = w >> 2;
  const int rb64 = xcd * 4 + (i & 3), cb80 = i >> 2, rb128 = xcd * 2 + (i & 1), cb96 = i >> 1;
  const unsigned w1g = WS_W1 + cb96 * 122880, w2g = WS_W2 + cb80 * 245760, l16 = lane * 16;
  auto w2_fetch = [&](int slot) {
    const int ts = slot / 10, r = slot - ts * 10, h = r >= 5;
    blds16(c.rs, l16, w2g + ((h * 24 + (ts)) * 5 + r - 5 * h) * 1024, lds + slot * 1024);
  };
  const f32x4 z4 = {0.f, 0.f, 0.f, 0.f};
  u32x4 e4[2];
  u32x2 e2, hres[5];
  rawbar();
  if (w < 6) {
#pragma unroll
    for (int q = 0; q < 6; q++) blds16(c.rs, l16, w1g + ((w) * 6 + q) * 1024, lds + L_W1 + (w * 6 + q) * 1024);
  }
  {
    const bf16* xr = p.x + (size_t)(rb64 * 64 + wm * 16 + r16) * D + cb80 * 80;
    ld16(e4[0], xr + g * 8); ld16(e4[1], xr + 32 + g * 8); ld8(e2, xr + 64 + g * 4);
  }
  if (w < 6) wait_vm<9>(); else wait_vm<3>();
  rawbar();
  raise(c, att_flag);
  STAMP(7);
  await(c, F_ATT + xcd * 32 + lane, 32, 0, 0);
  STAMP(8);
  {
    u32x4 a[10], bf[11];
    const unsigned ag = WS_O + ((rb64 * 4 + wm) * 20 + wk * 10) * 1024;
#pragma unroll
    for (int t = 0; t < 10; t++) ld_frag(a[t], c, ag + (t) * 1024);
    const unsigned l0 = (unsigned)(size_t)lds + L_WO + wk * 51200 + lane * 16, lb[3] = {l0, l0, l0};
    f32x4 acc[5] = {z4, z4, z4, z4, z4};
    k_steps<0, 10, 5, 5120, true, 50>(acc, a, bf, lb, [&](auto tc) { wait_reg<9 - tc.value>(a[tc.value]); });
    STAMP(9);
    asm volatile("" : "+v"(e4[0]), "+v"(e4[1]), "+v"(e2));
    epi80<true>(c, p, acc, e4, e2, hres, rb64, cb80);
  }
  rawbar();
  if (w < 4) wait_vm<0>();
  else if (w < 7) {
#pragma unroll 1
    for (int t = 2 + w; t < 20; t += 3) {
#pragma unroll
      for (int q = 0; q < 6; q++) blds16(c.rs, l16, w1g + ((t) * 6 + q) * 1024, lds + L_W1 + (t * 6 + q) * 1024);
    }
  }
  rawbar();
  raise(c, F_WO + rb64 * 8 + cb80);
  STAMP(10);
  wait_vm<0>();
  rawbar();
  LAS float* st_cs = (LAS float*)(lds + L_CS);
  if (w < 6) {
    LAS char* bl = lds + L_W1 + lane * 16;
    const u32x4 ones = {0x3f803f80u, 0x3f803f80u, 0x3f803f80u, 0x3f803f80u};
    f32x4 cs0 = z4, cs1 = z4;
#pragma unroll
    for (int t = 0; t < 20; t += 2) {
      cs0 = mma16(ones, *(LAS u32x4*)(bl + (t * 6 + w) * 1024), cs0);
      cs1 = mma16(ones, *(LAS u32x4*)(bl + (t * 6 + 6 + w) * 1024), cs1);
    }
    if (g == 0) st_cs[w * 16 + r16] = cs0[0] + cs1[0];
    asm volatile("s_waitcnt lgkmcnt(0)" ::: "memory");
  }
  await(c, F_WO + rb128 * 16 + lane, 16, 0, 0);
  STAMP(11);
  {
    u32x4 a[20], bf[12], bv[3];
    u32x2 sp0, sp1;
    const int row16 = rb128 * 8 + w;
    {
      const char* q = c.ws + WS_SP + row16 * 1024 + g * 256 + r16 * 8;
      asm volatile("global_load_dwordx2 %0, %2, off sc1\n\tglobal_load_dwordx2 %1, %2, off offset:128 sc1" : "=&v"(sp0), "=&v"(sp1) : "v"(q));
    }
    const unsigned ag = WS_H + row16 * 20480;
#pragma unroll
    for (int t = 0; t < WIN; t++) ld_frag(a[t], c, ag + t * 1024);
#pragma unroll
    for (int j = 0; j < 3; j++) ld16(bv[j], p.b1 + cb96 * 96 + j * 32 + g * 8);
    LAS char* bl = lds + L_W1 + lane * 16;
    f32x4 acc[6] = {z4, z4, z4, z4, z4, z4};
    const unsigned l0 = (unsigned)(size_t)bl, lb[3] = {l0, l0 + 61440, l0};
    asm volatile("s_waitcnt lgkmcnt(0)" ::: "memory");
    k_steps<0, 20, 6, 6144, true, 120>(acc, a, bf, lb, [&](auto tc) {
      constexpr int t = tc.value;
      if constexpr (t >= 1 && t + WIN - 1 < 20) ld_frag(a[t + WIN - 1], c, ag + (t + WIN - 1) * 1024);
      if constexpr (t >= 14 && t < 18) { const int s = w * 4 + t - 14; w2_fetch(s < 20 ? s : (s < 30 ? s + 130 : 159)); }
      wait_reg<(t < WIN ? 3 : 0) + (t + WIN - 1 < 20 ? WIN - 1 : 19 - t) + (t >= 14 ? (t < 18 ? t - 13 : 4) : 0)>(a[t]);
      stair<t, 20>();
    });
    STAMP(12);
    asm volatile("" : "+v"(sp0), "+v"(sp1), "+v"(bv[0]), "+v"(bv[1]), "+v"(bv[2]));
    const f32x2 q2 = __builtin_bit_cast(f32x2, sp0) + __builtin_bit_cast(f32x2, sp1);
    float s1 = q2[0], s2 = q2[1];
    s1 += __shfl_xor(s1, 16); s2 += __shfl_xor(s2, 16);
    s1 += __shfl_xor(s1, 32); s2 += __shfl_xor(s2, 32);
    const float mean = s1 * (1.f / D);
    const float rs = __builtin_amdgcn_rsqf(fmaxf(s2 * (1.f / D) - mean * mean, 0.f) + 1e-5f);
#pragma unroll
    for (int j = 0; j < 3; j++) {
      u32x4 o;
#pragma unroll
      for (int h = 0; h < 2; h++) {
        const int n = 2 * j + h;
        const f32x4 c4 = *(LAS f32x4*)(st_cs + n * 16 + g * 4);
        const float v0 = fmaxf(rs * (acc[n][0] - mean * c4[0]) + bflo(bv[j][2 * h]), 0.f);
        const float v1 = fmaxf(rs * (acc[n][1] - mean * c4[1]) + bfhi(bv[j][2 * h]), 0.f);
        const float v2 = fmaxf(rs * (acc[n][2] - mean * c4[2]) + bflo(bv[j][2 * h + 1]), 0.f);
        const float v3 = fmaxf(rs * (acc[n][3] - mean * c4[3]) + bfhi(bv[j][2 * h + 1]), 0.f);
        o[2 * h] = pk2(v0 * v0, v1 * v1); o[2 * h + 1] = pk2(v2 * v2, v3 * v3);
      }
      st16(c.rs, WS_G + (row16 * 48 + cb96 * 3 + j) * 1024 + lane * 16, o);
    }
  }
  bar();
  if (w < 5) {
#pragma unroll
    for (int q = 0; q < 10; q++) w2_fetch(20 + w * 10 + q);
  }
  static_for<0, 10>([&](auto qc) { wr_b<qc.value * 1024>((unsigned)(size_t)lds + (70 + w * 10) * 1024 + lane * 16, stash[qc.value]); });
  {
    const bf16* br = p.b2 + cb80 * 80;
    ld16(e4[0], br + g * 8); ld16(e4[1], br + 32 + g * 8); ld8(e2, br + 64 + g * 4);
  }
  if (w < 5) wait_vm<13>(); else wait_vm<3>();
  bar();
  raise(c, F_FF1 + rb128 * 16 + cb96);
  STAMP(13);
  wait_vm<0>();
  await(c, F_FF1 + (rb64 >> 1) * 16 + lane, 16, 0, 0);
  STAMP(14);
  {
    u32x4 a[24], bf[11];
    const unsigned ag = WS_G + ((rb64 * 4 + wm) * 48 + wk * 24) * 1024;
#pragma unroll
    for (int t = 0; t < WIN; t++) ld_frag(a[t], c, ag + t * 1024);
    auto wt = [&](auto tc) {
      constexpr int t = tc.value;
      if constexpr (t >= 1 && t + WIN - 1 < 24) ld_frag(a[t + WIN - 1], c, ag + (t + WIN - 1) * 1024);
      wait_reg<(t + WIN - 1 < 24 ? WIN - 1 : 23 - t)>(a[t]);
      stair<t % 8, 8>();
    };
    const unsigned l0 = (unsigned)(size_t)lds + wk * 5120 + lane * 16, lb[3] = {l0, l0 + 61440, l0 + 122880};
    f32x4 acc[5] = {z4, z4, z4, z4, z4};
    k_steps<0, 8, 5, 10240, true, 80>(acc, a, bf, lb, wt);
    rawbar();
    asm volatile("s_waitcnt lgkmcnt(0)" ::: "memory");
    static_for<0, 10>([&](auto qc) { wr_b<qc.value * 1024>((unsigned)(size_t)lds + w * 10240 + lane * 16, stash[10 + qc.value]); });
    k_steps<8, 16, 5, 10240, false, 80>(acc, a, bf, lb, wt);
    rawbar();
    k_steps<16, 24, 5, 10240, true, 120>(acc, a, bf, lb, wt);
    STAMP(15);
    asm volatile("" : "+v"(e4[0]), "+v"(e4[1]), "+v"(e2));
    epi80<false>(c, p, acc, e4, e2, hres, rb64, cb80);
  }
}

__device__ __forceinline__ f32x16 mma32(u32x4 a, u32x4 b, f32x16 c) {
  return __builtin_amdgcn_mfma_f32_32x32x16_bf16(__builtin_bit_cast(bf16x8, a), __builtin_bit_cast(bf16x8, b), c, 0, 0, 0);
}
__device__ __forceinline__ f32x16 mma32h(u32x2 a, u32x2 b, f32x16 c) {
  return __builtin_amdgcn_mfma_f32_32x32x8bf16_1k(__builtin_bit_cast(s16x4, a), __builtin_bit_cast(s16x4, b), c, 0, 0, 0);
}
__device__ __forceinline__ float max16(const f32x16& s) {
  float m = fmaxf(fmaxf(s[0], s[1]), s[2]);
#pragma unroll
  for (int i = 3; i < 15; i += 2) m = fmaxf(fmaxf(m, s[i]), s[i + 1]);
  return fmaxf(m, s[15]);
}
__device__ __forceinline__ u32x2 set_shift(u32x2 qhi, float m, int hh) {
  const float a = bflo(pk2(m, 0.f));
  const float r = m - a;
  const float b = bflo(pk2(r, 0.f));
  const u32x2 e = {pk2(-a, -b), pk2(b - r, 0.f)};
  return hh ? e : qhi;
}
__device__ __forceinline__ u32x2 exp4(const f32x16& s, int i) {
  const float e0 = __builtin_amdgcn_exp2f(s[i]), e1 = __builtin_amdgcn_exp2f(s[i + 1]);
  const float e2 = __builtin_amdgcn_exp2f(s[i + 2]), e3 = __builtin_amdgcn_exp2f(s[i + 3]);
  return (u32x2){pk2(e0, e1), pk2(e2, e3)};
}

template <int OFF> __device__ __forceinline__ void rd8(u32x2& v, unsigned ad) { asm volatile("ds_read_b64 %0, %1 offset:%2" : "=v"(v) : "v"(ad), "n"(OFF)); }
template <int OFF> __device__ __forceinline__ void rd16(u32x4& v, unsigned ad) { asm volatile("ds_read_b128 %0, %1 offset:%2" : "=v"(v) : "v"(ad), "n"(OFF)); }
__device__ __forceinline__ unsigned local_s(unsigned v) { asm volatile("" : "+s"(v)); return v; }
__device__ __forceinline__ void wait_k(u32x2& a, u32x2& b, u32x2& h, u32x4& v) {
  asm volatile("s_waitcnt lgkmcnt(2)" : "+v"(a), "+v"(b), "+v"(h), "+v"(v));
}
template <class T> __device__ __forceinline__ void pin(T& v) { asm volatile("" : "+v"(v)); }
__device__ __forceinline__ void ld_flags(u32x4& a, u32x4& b, const char* fp) {
  asm volatile("global_load_dwordx4 %0, %2, off sc1\n\tglobal_load_dwordx4 %1, %2, off offset:16 sc1" : "=&v"(a), "=&v"(b) : "v"(fp));
}

constexpr int SCHED[] = {300, 0, 1, 106, 107, 2, 3, 301, 4, 5, 100, 101, 6, 7, 302, 8, 9, 102, 103, 311, 10, 11, 303, 12, 13, 104, 105, 310, 14, 15};

__device__ __forceinline__ void attn_phase(Ctx& c, const Params& p, int head, int qb, int i, u32x4 (&stash)[20]) {
  LAS char* lds = c.lds;
  const int lane = c.lane, w = c.w;
  const int r = lane & 31, hh = lane >> 5;
  const int qrow = qb * 256 + w * 32 + r;
  const unsigned kvg = WS_KV + head * KV_HEAD;
  const char* qp = c.ws + WS_Q + (size_t)(head * 5 * S + qrow) * 8;
  u32x2 qa, qc, qhi0;
  asm volatile("global_load_dwordx2 %0, %1, off sc1" : "=v"(qa) : "v"(qp + (size_t)(2 * hh) * S * 8));
  asm volatile("global_load_dwordx2 %0, %1, off sc1" : "=v"(qc) : "v"(qp + (size_t)(2 * hh + 1) * S * 8));
  asm volatile("global_load_dwordx2 %0, %1, off sc1" : "=v"(qhi0) : "v"(qp + (size_t)4 * S * 8));
  auto issue1 = [&](int s, int j) {
    blds16(c.rs, lane * 16, kvg + s * AT_STAGE + (w + 8 * j) * 1024, lds + (s % 3) * AT_STAGE + (w + 8 * j) * 1024);
  };
  const int job = blockIdx.x * 3 + w;
  LAS char* jl = lds + 2 * AT_STAGE + w * 6144;
  issue1(0, 0); issue1(0, 1);
  wait_vm<0>();
  asm volatile("" : "+v"(qa), "+v"(qc), "+v"(qhi0));
  const u32x4 qlo = {qa[0], qa[1], qc[0], qc[1]};
  bar();
  STAMP(5);
  issue1(0, 2); issue1(0, 3); issue1(0, 4); issue1(0, 5);
  if (w < 3) retile_load(c, p, job, jl);

  const int koff = hh * 512 + r * 8, khoff = 1024 + lane * 8, voff = 1536 + hh * 384 + (r <= 20 ? r : 21) * 16;
  const u32x2 qz = hh ? (u32x2){0u, 0u} : qhi0;
  const f32x16 zero = {0.f, 0.f, 0.f, 0.f, 0.f, 0.f, 0.f, 0.f, 0.f, 0.f, 0.f, 0.f, 0.f, 0.f, 0.f, 0.f};
  u32x2 qhi;
  f32x16 sc;
  {
    const u32x2 k0a = *(LAS u32x2*)(lds + koff), k0b = *(LAS u32x2*)(lds + koff + 256);
    const u32x4 k0 = {k0a[0], k0a[1], k0b[0], k0b[1]};
    const u32x2 kh0 = *(LAS u32x2*)(lds + khoff);
    const f32x16 s0 = mma32(k0, qlo, zero);
    float m = max16(mma32h(kh0, qz, s0));
    m = fmaxf(m, __shfl_xor(m, 32));
    qhi = set_shift(qhi0, m, hh);
    sc = mma32h(kh0, qhi, s0);
  }
  f32x16 o = zero;
  const unsigned l0 = (unsigned)(size_t)lds;
  u32x2 ka, kb, khi;
  u32x4 v0, v1;
  rd8<KV_TILE>(ka, l0 + koff); rd8<KV_TILE + 256>(kb, l0 + koff); rd8<KV_TILE>(khi, l0 + khoff);
  rd16<0>(v0, l0 + voff); rd16<768>(v1, l0 + voff);
  u32x4 p1p = {0u, 0u, 0u, 0u}, v1p = p1p, fr0 = p1p, fr1 = p1p;
  const int cb80 = i >> 2;
  unsigned ak[2], ah[2], av[2];
  float el[4] = {0.f, 0.f, 0.f, 0.f};
  auto slot = [&](auto tc, auto lc) {
    constexpr int t = tc.value;
    constexpr bool LAST = lc.value;
    constexpr int xk = t + 2 >= 16, ok = ((t + 2) & 15) * KV_TILE, xv = t + 1 >= 16, ov = ((t + 1) & 15) * KV_TILE;
    if constexpr (t == 14 && !LAST) __builtin_amdgcn_s_setprio(3);
    if constexpr (t == 4) __builtin_amdgcn_s_setprio(2);
    if constexpr (t == 9) __builtin_amdgcn_s_setprio(1);
    if constexpr (t == 12) __builtin_amdgcn_s_setprio(0);
    f32x16 sn;
    float e[16];
    unsigned pp[6];
    static_for<0, sizeof(SCHED) / sizeof(int)>([&](auto oc) {
      constexpr int op = SCHED[oc.value];
      if constexpr (op < 16) { e[op] = __builtin_amdgcn_exp2f(sc[op]); if constexpr (op >= 12) pin(e[op]); }
      else if constexpr (op < 106) { pp[op - 100] = pk2(e[2 * (op - 100)], e[2 * (op - 100) + 1]); pin(pp[op - 100]); }
      else if constexpr (op < 108) p1p[op - 104] = pk2(el[2 * (op - 106)], el[2 * (op - 106) + 1]);
      else if constexpr (op == 300) {
        sn = mma32((u32x4){ka[0], ka[1], kb[0], kb[1]}, qlo, zero);
      } else if constexpr (op == 301) {
        sn = mma32h(khi, qhi, sn);
        SB();
        rd8<ok>(ka, ak[xk]); rd8<ok + 256>(kb, ak[xk]); rd8<ok>(khi, ah[xk]);
      } else if constexpr (op == 302) {
        o = mma32(v1p, p1p, o);
      } else if constexpr (op == 310) {
        wait_k(ka, kb, khi, v1p);
      } else if constexpr (op == 311) {
        wait_lds<4>(v0);
      } else {
        o = mma32(v0, (u32x4){pp[0], pp[1], pp[2], pp[3]}, o);
        SB();
        v1p = v1;
        rd16<ov>(v0, av[xv]); rd16<ov + 768>(v1, av[xv]);
      }
      SB();
    });
    p1p[0] = pp[4]; p1p[1] = pp[5];
    el[0] = e[12]; el[1] = e[13]; el[2] = e[14]; el[3] = e[15];
    sc = sn;
  };
  auto warm = [&](unsigned voff, unsigned soff) {
    __builtin_amdgcn_raw_ptr_buffer_load_lds(c.rs, (LAS void*)(lds + 159744), 4, voff, soff, 0, 16);
  };
  const unsigned w1h = WS_W1 + (i >> 1) * 122880 + (i & 1) * 61440, w2p = WS_W2 + cb80 * 245760;
  const unsigned w2w = w2p + w * 5120;
  const unsigned w1v = w * 8192 + lane * 128 < 61440 ? w * 8192 + lane * 128 : 0;
  __builtin_amdgcn_s_setprio(3);
  wait_k(ka, kb, khi, v1p);
#pragma unroll 1
  for (int s = 0; s < 2; s++) {
    const unsigned cb = l0 + s * AT_STAGE, nb = cb + AT_STAGE;
    ak[0] = cb + koff; ak[1] = nb + koff; ah[0] = cb + khoff; ah[1] = nb + khoff; av[0] = cb + voff; av[1] = nb + voff;
    static_for<0, 16>([&](auto tc) {
      constexpr int t = tc.value;
      if constexpr (t < 6) { const unsigned o8 = local_s(s + 1) * AT_STAGE + (w + 8 * t) * 1024; blds16(c.rs, lane * 16, kvg + o8, lds + o8); }
      if constexpr (t == 3) { if (s == 0) { if (w < 3) wait_vm<9>(); else wait_vm<4>(); rawbar(); } }
      if constexpr (t == 9) { if (s == 0 && w < 3) { wait_vm<0>(); retile_store(c, p, local_s(job), jl); } }
      if constexpr (t == 9) { if (s == 1 && w == 7) ld_flags(fr0, fr1, c.ws + WS_FLAG + (F_RT + lane * 4) * 8); }
      if constexpr (t == 14) {
        CYC3(23);
        wait_vm<0>();
        if (s == 1 && w == 7) {
          wait_reg<0>(fr0); wait_reg<0>(fr1);
          const unsigned lo = (unsigned)c.token, hi = (unsigned)(c.token >> 32);
          if (!__all(fr0[0] == lo && fr0[1] == hi && fr0[2] == lo && fr0[3] == hi && fr1[0] == lo && fr1[1] == hi && fr1[2] == lo && fr1[3] == hi)) {
            spin_flags(c, F_RT + lane, 64, F_RT + 64 + lane, 64);
            spin_flags(c, F_RT + 128 + lane, 64, F_RT + 192 + lane, 64);
          }
        }
        rawbar();
        CYC3(24);
        if (s == 0) raise(c, F_RT + blockIdx.x);
      }
      slot(tc, std::false_type{});
    });
  }
  ak[0] = ak[1]; ah[0] = ah[1]; av[0] = av[1];
  ak[1] = l0 + koff; ah[1] = l0 + khoff; av[1] = l0 + voff;
  static_for<0, 16>([&](auto tc) {
    constexpr int t = tc.value;
    if constexpr (t < 6) issue1(3, t);
    else {
      constexpr int q0 = 2 * (t - 6), q1 = q0 + 1;
      ld_frag(stash[q0], c, w2w + (((q0 / 5 & 1) * 24 + (q0 < 10 ? 7 : 16)) * 5 + q0 % 5) * 1024);
      ld_frag(stash[q1], c, w2w + (((q1 / 5 & 1) * 24 + (q1 < 10 ? 7 : 16)) * 5 + q1 % 5) * 1024);
      if constexpr (t % 2 == 1 && t < 14) { if (w == (t - 7) / 2) warm(w1v, w1h); }
    }
    if constexpr (t == 14) { CYC(27); wait_vm<18>(); rawbar(); CYC(28); }
    SB();
    slot(tc, std::false_type{});
  });
  ak[0] = ak[1]; ah[0] = ah[1]; av[0] = av[1];
  static_for<0, 16>([&](auto tc) {
    constexpr int t = tc.value;
    if constexpr (t == 2) { CYC(29); wait_vm<0>(); rawbar(); CYC(30); }
    if constexpr (t >= 2 && t < 15) {
      if (t < 14 || w < 4) blds16(c.rs, lane * 16, WS_WO + cb80 * 102400 + (w + 8 * (t - 2)) * 1024, lds + L_WO + (w + 8 * (t - 2)) * 1024);
      if constexpr (t % 3 == 0) { if (w == 3 + t / 3) __builtin_amdgcn_raw_ptr_buffer_load_lds(c.rs, (LAS void*)(lds + AT_STAGE + w * 256), 4, w1v, w1h, 0, 16); }
      if constexpr (t % 3 == 1 && t < 13) { if (w == t / 3 - 1) __builtin_amdgcn_raw_ptr_buffer_load_lds(c.rs, (LAS void*)(lds + AT_STAGE + w * 256), 4, (w * 64 + lane < 140 ? w * 64 + lane : 0) * 128, w2p + (i & 1) * 122880 + ((i >> 1) & 1) * 17920, 0, 16); }
    }
    SB();
    slot(tc, std::true_type{});
  });
  __builtin_amdgcn_s_setprio(0);
  wait_lds<0>(v1p); wait_lds<0>(ka); wait_lds<0>(kb); wait_lds<0>(khi); wait_lds<0>(v0); wait_lds<0>(v1);
  p1p[2] = pk2(el[0], el[1]); p1p[3] = pk2(el[2], el[3]);
  o = mma32(v1p, p1p, o);
  STAMP(6);
  float l = __shfl(o[8], r + 32);
  if (!__all(l < 1.8446744e19f)) {
    const unsigned kb = WS_KV + head * KV_HEAD;
    auto gk = [&](int t) {
      const u32x2 a = __builtin_amdgcn_raw_buffer_load_b64(c.rs, kb + t * KV_TILE + koff, 0, 16);
      const u32x2 b = __builtin_amdgcn_raw_buffer_load_b64(c.rs, kb + t * KV_TILE + koff + 256, 0, 16);
      return mma32((u32x4){a[0], a[1], b[0], b[1]}, qlo, zero);
    };
    float m = -__builtin_inff();
#pragma unroll 1
    for (int t = 0; t < 64; t++)
      m = fmaxf(m, max16(mma32h(__builtin_amdgcn_raw_buffer_load_b64(c.rs, kb + t * KV_TILE + khoff, 0, 16), qz, gk(t))));
    m = fmaxf(m, __shfl_xor(m, 32));
    qhi = set_shift(qhi0, m, hh);
    o = zero;
#pragma unroll 1
    for (int t = 0; t < 64; t++) {
      const f32x16 s2 = mma32h(__builtin_amdgcn_raw_buffer_load_b64(c.rs, kb + t * KV_TILE + khoff, 0, 16), qhi, gk(t));
      const u32x2 pa = exp4(s2, 0), pb = exp4(s2, 4), pc = exp4(s2, 8), pd = exp4(s2, 12);
      o = mma32(__builtin_amdgcn_raw_buffer_load_b128(c.rs, kb + t * KV_TILE + voff, 0, 16), (u32x4){pa[0], pa[1], pb[0], pb[1]}, o);
      o = mma32(__builtin_amdgcn_raw_buffer_load_b128(c.rs, kb + t * KV_TILE + voff + 768, 0, 16), (u32x4){pc[0], pc[1], pd[0], pd[1]}, o);
    }
    l = __shfl(o[8], r + 32);
  }
  const float inv = 1.f / l;
  const auto x0 = __builtin_amdgcn_permlane32_swap(pk2(o[0] * inv, o[1] * inv), pk2(o[4] * inv, o[5] * inv), false, false);
  const auto x1 = __builtin_amdgcn_permlane32_swap(pk2(o[2] * inv, o[3] * inv), pk2(o[6] * inv, o[7] * inv), false, false);
  const int ca = head * DH + (head & 1) * 4 + hh * 8, cr = head * DH + ((head & 1) ? 0 : 16);
  const unsigned ob = WS_O + (qrow >> 4) * 20480 + (qrow & 15) * 16;
  st16(c.rs, ob + (ca >> 3) * 256, (u32x4){x0[0], x1[0], x0[1], x1[1]});
  if (hh == 0) st8(c.rs, ob + (cr >> 3) * 256 + (cr & 4) * 2, (u32x2){pk2(o[8] * inv, o[9] * inv), pk2(o[10] * inv, o[11] * inv)});
}

__global__ __launch_bounds__(512, 2) void k_block(Params p) {
  extern __shared__ __attribute__((aligned(16))) char smem[];
  Ctx c;
  c.tid = threadIdx.x; c.lane = c.tid & 63; c.w = __builtin_amdgcn_readfirstlane(c.tid >> 6);
  c.r16 = c.lane & 15; c.g = c.lane >> 4;
  c.lds = (LAS char*)smem; c.ws = p.ws;
  c.rs = __builtin_amdgcn_make_buffer_rsrc(p.ws, 0, WS_END, 0x00020000);
  c.rd = (u32x4){(unsigned)(size_t)p.ws, (unsigned)((size_t)p.ws >> 32) & 0xffffu, WS_END, 0x00020000u};
  STAMP(0);
  if (c.w == 7) {
    __builtin_amdgcn_raw_ptr_buffer_load_lds(c.rs, (LAS void*)(c.lds + 150016), 4, (c.lane * 6 + (blockIdx.x >> 3) % 6) * 65536, 0, 0, 0);
    const char* q = c.lane < 20 ? (const char*)p.out + c.lane * 131072 : c.lane < 27 ? (const char*)p.wo + (c.lane - 20) * 131072 : c.lane < 42 ? (const char*)p.w1 + (c.lane - 27) * 131072 : c.lane < 57 ? (const char*)p.w2 + (c.lane - 42) * 131072 : (const char*)p.b1;
    __builtin_amdgcn_global_load_lds((const GAS void*)q, (LAS void*)(c.lds + 150528), 4, 0, 0);
  }
  if (c.w == 6) {
    u64 pc;
    asm volatile("s_getpc_b64 %0" : "=s"(pc));
    const unsigned line = (blockIdx.x >> 3) + 32 * c.lane;
    if (line < 56000 / 128) __builtin_amdgcn_global_load_lds((const GAS void*)((pc & ~127ull) + line * 128), (LAS void*)(c.lds + 151040), 4, 0, 0);
  }
  const int b = blockIdx.x, xcd = b & 7, i = b >> 3;
  {
    const int rb = (xcd & 3) * 4 + (i & 3), cb = (xcd >> 2) * 8 + (i >> 2);
    if (cb < 10) qkv_phase<false>(c, p, rb, cb);
    else if (cb < 15) qkv_phase<true>(c, p, rb, cb);
    else {
      RowLd rl;
      row_load(rl, c, p, rb * 128 + 120 + c.w);
      if (c.w < 6) retile_load(c, p, 768 + rb * 6 + c.w, c.lds + c.w * 6144);
      wait_vm<0>();
      row_landed(rl, c);
      row_stats(rl, c, rb * 128 + 120 + c.w);
      if (c.w < 6) retile_store(c, p, 768 + rb * 6 + c.w, c.lds + c.w * 6144);
    }
    if (cb < 15) publish(c, F_QKV + cb * 16 + rb);
    STAMP(3);
  }
  const int head = xcd * 4 + (i & 3), qb = i >> 2;
  {
    const int c0 = head * 20 / 128, c1 = (head * 20 + 19) / 128, j = c.lane >> 4;
    await(c, F_QKV + (((j & 1) ? c1 : c0) + (j >> 1) * 5) * 16 + c.r16, 64, F_QKV + (((j & 1) ? c1 : c0) + 10) * 16 + c.r16, 32);
  }
  STAMP(4);
  u32x4 stash[20];
  attn_phase(c, p, head, qb, i, stash);
  tail_phases(c, p, xcd, i, F_ATT + qb * 32 + head, stash);
  if (b == 0 && c.tid == 0) __hip_atomic_store((u64*)(p.ws + WS_FLAG) + F_EPOCH, c.epoch + 1, __ATOMIC_RELAXED, __HIP_MEMORY_SCOPE_AGENT);
}

extern "C" void kernel_launch(void* const* d_in, const int* in_sizes, int n_in, void* d_out, int out_size, void* d_ws,
                              size_t ws_size, hipStream_t stream) {
  Params p;
  p.x = (const bf16*)d_in[0]; p.wqkv = (const bf16*)d_in[1]; p.wo = (const bf16*)d_in[2];
  p.w1 = (const bf16*)d_in[4]; p.b1 = (const bf16*)d_in[5]; p.w2 = (const bf16*)d_in[6]; p.b2 = (const bf16*)d_in[7];
  p.out = (bf16*)d_out; p.ws = (char*)d_ws;
  (void)hipFuncSetAttribute((const void*)k_block, hipFuncAttributeMaxDynamicSharedMemorySize, LDS_BYTES);
  k_block<<<256, 512, LDS_BYTES, stream>>>(p);
}
```

```cpp
#include <hip/hip_runtime.h>
#include <hip/hip_bf16.h>
#include <stdint.h>
#include <type_traits>

#define LAS __attribute__((address_space(3)))
#define GAS __attribute__((address_space(1)))
typedef __bf16 bf16;
typedef unsigned long long u64;
typedef __bf16 bf16x2 __attribute__((ext_vector_type(2)));
typedef __bf16 bf16x8 __attribute__((ext_vector_type(8)));
typedef short s16x4 __attribute__((ext_vector_type(4)));
typedef float f32x2 __attribute__((ext_vector_type(2)));
typedef float f32x4 __attribute__((ext_vector_type(4)));
typedef float f32x16 __attribute__((ext_vector_type(16)));
typedef unsigned u32x2 __attribute__((ext_vector_type(2)));
typedef unsigned u32x4 __attribute__((ext_vector_type(4)));
typedef __amdgpu_buffer_rsrc_t rsrc_t;

constexpr int S = 2048, D = 640, NH = 32, DH = 20, FF = 1536;
constexpr int KV_TILE = 3072;
constexpr int KV_HEAD = 64 * KV_TILE;
constexpr int AT_STAGE = 16 * KV_TILE;
constexpr int LDS_BYTES = 163840;
constexpr unsigned WS_FLAG = 0;
constexpr unsigned F_QKV = 0, F_CL = 256, F_RT = 1024, F_EPOCH = 1536;
constexpr unsigned WS_Q = 16384;
constexpr unsigned WS_KV = WS_Q + NH * S * 40;
constexpr unsigned WS_O = WS_KV + NH * KV_HEAD;
constexpr unsigned WS_H = WS_O + S * D * 2;
constexpr unsigned WS_G = WS_H + S * D * 2;
constexpr unsigned WS_WO = WS_G + S * FF * 2;
constexpr unsigned WS_W1 = WS_WO + D * D * 2;
constexpr unsigned WS_W2 = WS_W1 + D * FF * 2;
constexpr unsigned WS_SP = WS_W2 + D * FF * 2;
constexpr unsigned WS_RS = WS_SP + S * 64;
constexpr unsigned WS_CP = WS_RS + S * 16;
constexpr unsigned WS_END = WS_CP + 15 * 10 * 1024;

#define SB() __builtin_amdgcn_sched_barrier(0)
template <int N> __device__ __forceinline__ void wait_vm() { asm volatile("s_waitcnt vmcnt(%0)" ::"n"(N) : "memory"); }
__device__ __forceinline__ void bar() { asm volatile("s_waitcnt lgkmcnt(0)" ::: "memory"); __builtin_amdgcn_s_barrier(); asm volatile("" ::: "memory"); }
__device__ __forceinline__ void rawbar() { asm volatile("" ::: "memory"); __builtin_amdgcn_s_barrier(); asm volatile("" ::: "memory"); }
template <int AUX> __device__ __forceinline__ void glds16(const void* g, LAS char* l) {
  __builtin_amdgcn_global_load_lds((const GAS void*)g, (LAS void*)l, 16, 0, AUX);
}
__device__ __forceinline__ void blds16(rsrc_t r, unsigned voff, unsigned soff, LAS char* l) {
  __builtin_amdgcn_raw_ptr_buffer_load_lds(r, (LAS void*)l, 16, voff, soff, 0, 16);
}
__device__ __forceinline__ void st16(rsrc_t r, unsigned off, u32x4 v) { __builtin_amdgcn_raw_buffer_store_b128(v, r, off, 0, 16); }
__device__ __forceinline__ void st8(rsrc_t r, unsigned off, u32x2 v) { __builtin_amdgcn_raw_buffer_store_b64(v, r, off, 0, 16); }
__device__ __forceinline__ unsigned pk2(float a, float b) {
  f32x2 f = {a, b};
  return __builtin_bit_cast(unsigned, __builtin_convertvector(f, bf16x2));
}
__device__ __forceinline__ float bflo(unsigned u) { return __builtin_bit_cast(float, u << 16); }
__device__ __forceinline__ float bfhi(unsigned u) { return __builtin_bit_cast(float, u & 0xffff0000u); }
__device__ __forceinline__ f32x4 mma16(u32x4 a, u32x4 b, f32x4 c) {
  return __builtin_amdgcn_mfma_f32_16x16x32_bf16(__builtin_bit_cast(bf16x8, a), __builtin_bit_cast(bf16x8, b), c, 0, 0, 0);
}
template <int I, int N, class F> __device__ __forceinline__ void static_for(F&& f) {
  if constexpr (I < N) { f(std::integral_constant<int, I>{}); static_for<I + 1, N>(f); }
}

struct Params {
  const bf16 *x, *wqkv, *wo, *w1, *b1, *w2, *b2;
  bf16* out;
  char* ws;
};
struct Ctx {
  int tid, lane, w, r16, g;
  LAS char* lds;
  char* ws;
  rsrc_t rs;
  u32x4 rd;
  u64 token, epoch;
};
#define CYC(i) do {} while (0)
#define CYC3(i) do {} while (0)
#define STAMP(i) do {} while (0)

__device__ __forceinline__ void publish(Ctx& c, unsigned flag) {
  wait_vm<0>();
  bar();
  if (c.tid == 0) __hip_atomic_store((u64*)(c.ws + WS_FLAG) + flag, c.token, __ATOMIC_RELAXED, __HIP_MEMORY_SCOPE_AGENT);
}
__device__ __forceinline__ void spin_flags(Ctx& c, unsigned idx0, int n0, unsigned idx1, int n1) {
  const u64* f = (const u64*)(c.ws + WS_FLAG);
  for (int it = 0; it < (1 << 20); it++) {
    u64 a = c.token, b = c.token;
    if (c.lane < n0) a = __hip_atomic_load(f + idx0, __ATOMIC_RELAXED, __HIP_MEMORY_SCOPE_AGENT);
    if (c.lane < n1) b = __hip_atomic_load(f + idx1, __ATOMIC_RELAXED, __HIP_MEMORY_SCOPE_AGENT);
    if (__all(a == c.token && b == c.token)) break;
    __builtin_amdgcn_s_sleep(1);
  }
}
__device__ __forceinline__ void await(Ctx& c, unsigned idx0, int n0, unsigned idx1, int n1) {
  if (c.w == 7) spin_flags(c, idx0, n0, idx1, n1);
  rawbar();
}

struct RowLd { u32x4 a; unsigned b; u64 ep; };
__device__ __forceinline__ void row_load(RowLd& r, const Ctx& c, const Params& p, int row) {
  const char* x = (const char*)(p.x + (size_t)row * D);
  asm volatile("global_load_dwordx2 %0, %3, off sc1\n\tglobal_load_dwordx4 %1, %4, off\n\tglobal_load_dword %2, %5, off"
               : "=&v"(r.ep), "=&v"(r.a), "=&v"(r.b) : "v"(p.ws + WS_FLAG + F_EPOCH * 8), "v"(x + c.lane * 16), "v"(x + 1024 + c.lane * 4));
}
__device__ __forceinline__ void row_landed(RowLd& r, Ctx& c) {
  asm volatile("" : "+v"(r.ep), "+v"(r.a), "+v"(r.b));
  c.epoch = ((u64)__builtin_amdgcn_readfirstlane((unsigned)(r.ep >> 32)) << 32) | __builtin_amdgcn_readfirstlane((unsigned)r.ep);
  const u64 z = (c.epoch + 0x9e3779b97f4a7c15ull) * 0xbf58476d1ce4e5b9ull;
  c.token = z ^ (z >> 31);
}
__device__ __forceinline__ float wave_sum(float v) {
  v += __builtin_bit_cast(float, __builtin_amdgcn_update_dpp(0, __builtin_bit_cast(int, v), 0x128, 0xf, 0xf, false));
  v += __builtin_bit_cast(float, __builtin_amdgcn_update_dpp(0, __builtin_bit_cast(int, v), 0x124, 0xf, 0xf, false));
  v += __builtin_bit_cast(float, __builtin_amdgcn_update_dpp(0, __builtin_bit_cast(int, v), 0x122, 0xf, 0xf, false));
  v += __builtin_bit_cast(float, __builtin_amdgcn_update_dpp(0, __builtin_bit_cast(int, v), 0x121, 0xf, 0xf, false));
  return v;
}
__device__ __forceinline__ float rows_sum(float v) {
  const unsigned u = __builtin_bit_cast(unsigned, v);
  const auto s = __builtin_amdgcn_permlane16_swap(u, u, false, false);
  const unsigned s0 = s[0], s1 = s[1];
  const unsigned m = __builtin_bit_cast(unsigned, __builtin_bit_cast(float, s0) + __builtin_bit_cast(float, s1));
  const auto q = __builtin_amdgcn_permlane32_swap(m, m, false, false);
  const unsigned q0 = q[0], q1 = q[1];
  return __builtin_bit_cast(float, q0) + __builtin_bit_cast(float, q1);
}
__device__ __forceinline__ void row_stats(const RowLd& r, const Ctx& c, int row) {
  float s1 = bflo(r.b) + bfhi(r.b), s2 = bflo(r.b) * bflo(r.b) + bfhi(r.b) * bfhi(r.b);
#pragma unroll
  for (int j = 0; j < 4; j++) { const float lo = bflo(r.a[j]), hi = bfhi(r.a[j]); s1 += lo + hi; s2 += lo * lo + hi * hi; }
  const float mean = rows_sum(wave_sum(s1)) * (1.f / D);
  const float rstd = __builtin_amdgcn_rsqf(fmaxf(rows_sum(wave_sum(s2)) * (1.f / D) - mean * mean, 0.f) + 1e-5f);
  const unsigned tag = (unsigned)c.token;
  if (c.lane == 0) st16(c.rs, WS_RS + row * 16, (u32x4){__builtin_bit_cast(unsigned, mean), tag, __builtin_bit_cast(unsigned, rstd), tag});
}

__device__ __forceinline__ void gather(u32x4& s, u32x2 (&q)[3], const char* sp, const char* cp, int o2) {
  asm volatile("global_load_dwordx4 %0, %4, off sc1\n\tglobal_load_dwordx2 %1, %5, off sc1\n\tglobal_load_dwordx2 %2, %6, off sc1\n\tglobal_load_dwordx2 %3, %7, off sc1"
               : "=&v"(s), "=&v"(q[0]), "=&v"(q[1]), "=&v"(q[2]) : "v"(sp), "v"(cp), "v"(cp + 4096), "v"(cp + o2));
}
template <bool ISV>
__device__ __forceinline__ void qkv_phase(Ctx& c, const Params& p, int rb, int cb) {
  constexpr int NS = 4, NKT = 10, STAGE = 32768, LDB = 3 * D;
  LAS char* lds = c.lds;
  LAS float* st_mean = (LAS float*)(lds + NS * STAGE);
  LAS float* st_rstd = st_mean + 128;
  LAS float* st_cs = st_rstd + 128;
  const int lane = c.lane, w = c.w, r16 = c.r16, g = c.g;
  const int wm = w & 3, wn = w >> 2;
  const int row0 = rb * 128, col0 = cb * 128;
  const int rot = (rb + cb) % NKT;

  const char* srcA[2];
  const char* srcB[2];
#pragma unroll
  for (int i = 0; i < 2; i++) {
    const int pc = w + 8 * i;
    {
      const int lr = pc * 8 + (lane >> 3), rho = lr & 31;
      const int gr = ISV ? (16 * ((rho >> 3) & 1) + 8 * (rho >> 4) + (rho & 7)) : (2 * (rho & 15) + (rho >> 4));
      const int ch = (lane & 7) ^ ((lr >> 1) & 7);
      srcA[i] = (const char*)(p.x + (size_t)(row0 + (lr & ~31) + gr) * D + ch * 8);
    }
    {
      const int row = lane >> 3, f = ((row >> 1) & 1) | ((w & 1) << 1);
      const int ch = (lane & 7) ^ (f << 1);
      srcB[i] = (const char*)(p.wqkv + (size_t)(w * 8 + row) * LDB + col0 + i * 64 + ch * 8);
    }
  }
  auto issue = [&](int t, int i) {
    int kt = t + rot;
    if (kt >= NKT) kt -= NKT;
    LAS char* sb = lds + (t % NS) * STAGE;
    if (i < 2) glds16<0>(srcA[i] + kt * 128, sb + (w + 8 * i) * 1024);
    else glds16<0>(srcB[i - 2] + (size_t)kt * (64 * LDB * 2), sb + 16384 + (w + 8 * (i - 2)) * 1024);
  };
  RowLd rl;
  row_load(rl, c, p, row0 + cb * 8 + w);
#pragma unroll
  for (int i = 0; i < 4; i++) issue(0, i);

  int aoff[2], boff[4];
#pragma unroll
  for (int m = 0; m < 2; m++) {
    const int row = wm * 32 + (m ^ wn) * 16 + r16;
    aoff[m] = row * 128 + ((g ^ ((row >> 1) & 7)) << 4);
  }
#pragma unroll
  for (int n = 0; n < 4; n++) {
    const int q = (lane >> 2) & 3, pp = lane & 3, f = ((q >> 1) & 1) | ((g & 1) << 1);
    const int nt = (n + wm) & 3;
    boff[n] = 16384 + wn * 8192 + (g * 8 + q) * 128 + (((nt * 2 + (pp >> 1)) ^ (f << 1)) << 4) + (pp & 1) * 8;
  }
  f32x4 acc[2][4], a_cs = {0.f, 0.f, 0.f, 0.f};
  u32x4 gs;
  u32x2 gc[3];
  const int rho = (w & 1) * 16 + r16;
  const char* gsp = c.ws + WS_RS + (row0 + (w >> 1) * 32 + (ISV ? 16 * ((rho >> 3) & 1) + 8 * (rho >> 4) + (rho & 7) : 2 * r16 + (w & 1))) * 16;
  const char* gcp = c.ws + WS_CP + (cb * 10 + g) * 1024 + (w * 16 + r16) * 8;
  const int g2 = g < 2 ? 8192 : 0;
#pragma unroll
  for (int m = 0; m < 2; m++)
#pragma unroll
    for (int n = 0; n < 4; n++) acc[m][n] = a_cs;
  const u32x4 ones = {0x3f803f80u, 0x3f803f80u, 0x3f803f80u, 0x3f803f80u};
  u32x4 af[2][2][2];
  u32x2 bq[2][2][4][2];
  auto read1 = [&](int t, int r) {
    LAS char* sb = lds + (t % NS) * STAGE;
    const int bi = t & 1;
    if (r < 8) {
      const int kk = r >> 2, n = r & 3;
      const unsigned ad = (unsigned)(size_t)(sb + boff[n]);
      if (kk == 0) asm volatile("ds_read_b64_tr_b16 %0, %2\n\tds_read_b64_tr_b16 %1, %2 offset:512"
                                : "=&v"(bq[bi][0][n][0]), "=&v"(bq[bi][0][n][1]) : "v"(ad));
      else asm volatile("ds_read_b64_tr_b16 %0, %2 offset:4096\n\tds_read_b64_tr_b16 %1, %2 offset:4608"
                        : "=&v"(bq[bi][1][n][0]), "=&v"(bq[bi][1][n][1]) : "v"(ad));
    } else {
      const int kk = (r - 8) >> 1, m = r & 1;
      af[bi][kk][m] = *(LAS u32x4*)(sb + (aoff[m] ^ (kk << 6)));
    }
  };
#pragma unroll
  for (int i = 0; i < 4; i++) issue(1, i);
  wait_vm<4>();
  row_landed(rl, c);
  bar();
  STAMP(1);
#pragma unroll
  for (int r = 0; r < 12; r++) { read1(0, r); if (r < 8) issue(2 + (r >> 2), r & 3); }
  static_for<0, NKT>([&](auto tc) {
    constexpr int t = tc.value, bi = t & 1;
    if constexpr (t + 1 < NKT) wait_vm<4 * ((NKT - 2 - t) < (NS - 2) ? (NKT - 2 - t) : (NS - 2))>();
    asm volatile("s_waitcnt lgkmcnt(0)"
                 : "+v"(bq[bi][0][0][0]), "+v"(bq[bi][0][0][1]), "+v"(bq[bi][1][0][0]), "+v"(bq[bi][1][0][1]),
                   "+v"(bq[bi][0][1][0]), "+v"(bq[bi][0][1][1]), "+v"(bq[bi][1][1][0]), "+v"(bq[bi][1][1][1]),
                   "+v"(bq[bi][0][2][0]), "+v"(bq[bi][0][2][1]), "+v"(bq[bi][1][2][0]), "+v"(bq[bi][1][2][1]),
                   "+v"(bq[bi][0][3][0]), "+v"(bq[bi][0][3][1]), "+v"(bq[bi][1][3][0]), "+v"(bq[bi][1][3][1]));
    if constexpr (t + 1 < NKT) bar();
    SB();
#pragma unroll
    for (int i = 0; i < 16; i++) {
      const int kk = i >> 3, m = (i >> 2) & 1, n = i & 3;
      const u32x4 bv = {bq[bi][kk][n][0][0], bq[bi][kk][n][0][1], bq[bi][kk][n][1][0], bq[bi][kk][n][1][1]};
      acc[m][n] = ISV ? mma16(af[bi][kk][m], bv, acc[m][n]) : mma16(bv, af[bi][kk][m], acc[m][n]);
      if (t == 0 && (i & 7) == 0) a_cs = mma16(ones, bv, a_cs);
      if (t + 1 < NKT && i < 12) read1(t + 1, i < 4 ? i : (i < 6 ? i + 4 : (i < 10 ? i - 2 : i)));
      if (t + NS < NKT && (i & 3) == 2) issue(t + NS, i >> 2);
      if (t == 0 && i == 13) row_stats(rl, c, row0 + cb * 8 + w);
      if (t == 1 && i == 13 && rb < 10 && g == 0)
        st8(c.rs, WS_CP + (cb * 10 + rot) * 1024 + ((wn * 4 + wm) * 16 + r16) * 8, (u32x2){__builtin_bit_cast(unsigned, a_cs[0] + 0.f), (unsigned)c.token});
      if (t == 8 && i == 1) gather(gs, gc, gsp, gcp, g2);
      SB();
    }
  });
  STAMP(2);
  {
    const unsigned tag = (unsigned)c.token;
    for (int it = 0; it < (1 << 20); it++) {
      asm volatile("s_waitcnt vmcnt(0)" : "+v"(gs), "+v"(gc[0]), "+v"(gc[1]), "+v"(gc[2]));
      if (__all(gs[1] == tag && gs[3] == tag && gc[0][1] == tag && gc[1][1] == tag && gc[2][1] == tag)) break;
      gather(gs, gc, gsp, gcp, g2);
    }
    const unsigned c0 = gc[0][0], c1 = gc[1][0], c2 = gc[2][0], m0 = gs[0], r0 = gs[2];
    const float cs = rows_sum(__builtin_bit_cast(float, c0) + __builtin_bit_cast(float, c1) + (g < 2 ? __builtin_bit_cast(float, c2) : 0.f));
    if (g == 0) {
      st_mean[w * 16 + r16] = __builtin_bit_cast(float, m0);
      st_rstd[w * 16 + r16] = __builtin_bit_cast(float, r0);
      st_cs[w * 16 + r16] = cs;
    }
    bar();
  }
  const int tile = (row0 >> 5) + wm;
  if (!ISV) {
    const bool isq = cb < 5;
    float mean[2], rs[2];
#pragma unroll
    for (int m = 0; m < 2; m++) {
      mean[m] = st_mean[wm * 32 + (m ^ wn) * 16 + r16];
      rs[m] = st_rstd[wm * 32 + (m ^ wn) * 16 + r16] * (isq ? 0.22360679774997896f * 1.4426950408889634f : 1.f);
    }
#pragma unroll
    for (int n = 0; n < 4; n++) {
      const int lc = (wn * 4 + ((n + wm) & 3)) * 16 + g * 4;
      const f32x4 c4 = *(LAS f32x4*)(st_cs + lc);
      u32x2 o[2];
#pragma unroll
      for (int m = 0; m < 2; m++) {
        o[m][0] = pk2(rs[m] * (acc[m][n][0] - mean[m] * c4[0]), rs[m] * (acc[m][n][1] - mean[m] * c4[1]));
        o[m][1] = pk2(rs[m] * (acc[m][n][2] - mean[m] * c4[2]), rs[m] * (acc[m][n][3] - mean[m] * c4[3]));
      }
      const u32x4 v = wn ? (u32x4){o[1][0], o[1][1], o[0][0], o[0][1]} : (u32x4){o[0][0], o[0][1], o[1][0], o[1][1]};
      const unsigned cc = col0 + lc - (isq ? 0 : D);
      if (isq) {
        st16(c.rs, WS_Q + ((cc >> 2) * S + tile * 32 + 2 * r16) * 8, v);
      } else {
        const unsigned head = cc / 20u, d0 = cc - head * 20u;
        const unsigned t = WS_KV + head * KV_HEAD + tile * KV_TILE + r16 * 16;
        st16(c.rs, t + d0 * 64, v);
        if (d0 == 16) st16(c.rs, t + 1280, (u32x4){0x3f803f80u, 0x00003f80u, 0x3f803f80u, 0x00003f80u});
      }
    }
  } else {
    f32x4 mean[2], rs[2];
#pragma unroll
    for (int m = 0; m < 2; m++) {
      mean[m] = *(LAS f32x4*)(st_mean + wm * 32 + (m ^ wn) * 16 + g * 4);
      rs[m] = *(LAS f32x4*)(st_rstd + wm * 32 + (m ^ wn) * 16 + g * 4);
    }
#pragma unroll
    for (int n = 0; n < 4; n++) {
      const int lc = (wn * 4 + ((n + wm) & 3)) * 16 + r16;
      const float cs = st_cs[lc];
      u32x2 o[2];
#pragma unroll
      for (int m = 0; m < 2; m++) {
        o[m][0] = pk2(rs[m][0] * (acc[m][n][0] - mean[m][0] * cs), rs[m][1] * (acc[m][n][1] - mean[m][1] * cs));
        o[m][1] = pk2(rs[m][2] * (acc[m][n][2] - mean[m][2] * cs), rs[m][3] * (acc[m][n][3] - mean[m][3] * cs));
      }
      const u32x4 v = wn ? (u32x4){o[1][0], o[1][1], o[0][0], o[0][1]} : (u32x4){o[0][0], o[0][1], o[1][0], o[1][1]};
      const unsigned cc = col0 + lc - 2 * D;
      const unsigned head = cc / 20u, d = cc - head * 20u;
      const unsigned t = WS_KV + tile * KV_TILE + 1536 + g * 384, odd = head & 1;
      const unsigned row = odd ? (d < 4 ? d + 16 : d - 4) : d, e = (row & 3) ? 0u : 0x3f803f80u;
      const bool mix = row >= 16;
      const u32x4 cv = {e, e, e, e};
      st16(c.rs, t + (mix ? head & ~1u : head) * KV_HEAD + (mix && odd ? row + 4 : row) * 16, mix && odd ? cv : v);
      if (mix) st16(c.rs, t + (head | 1u) * KV_HEAD + (odd ? row : row + 4) * 16, odd ? v : cv);
    }
  }
}

__device__ __forceinline__ void retile_job(const Params& p, int j, const bf16*& W, unsigned& dst, int& cpr) {
  if (j < 160) { const int ks = j >> 3, blk = j & 7; W = p.wo + ks * 32 * D + blk * 80; dst = WS_WO + (blk * 20 + ks) * 5120; cpr = 10; }
  else if (j < 480) { const int e = j - 160, ks = e >> 4, blk = e & 15; W = p.w1 + ks * 32 * FF + blk * 96; dst = WS_W1 + (blk * 20 + ks) * 6144; cpr = 12; }
  else { const int e = j - 480, ks = e >> 3, blk = e & 7; W = p.w2 + ks * 32 * D + blk * 80; dst = WS_W2 + (blk * 48 + ks) * 5120; cpr = 10; }
}
__device__ __forceinline__ void retile_load(Ctx& c, const Params& p, int j, LAS char* base) {
  const bf16* W; unsigned dst; int cpr;
  retile_job(p, j, W, dst, cpr);
  const int ld = cpr == 12 ? FF : D;
#pragma unroll
  for (int i = 0; i < 6; i++) {
    const int q = i * 64 + c.lane, row = cpr == 12 ? q / 12 : q / 10, ch = q - row * cpr;
    if (i < 5 || cpr == 12) glds16<0>(W + row * ld + ch * 8, base + i * 1024);
  }
}
__device__ __forceinline__ void retile_store(Ctx& c, const Params& p, int j, LAS char* base) {
  const bf16* W; unsigned dst; int cpr;
  retile_job(p, j, W, dst, cpr);
  const int st = cpr * 16, pp = c.lane & 3;
  const unsigned ad = (unsigned)(size_t)base + (c.g * 8 + ((c.lane >> 2) & 3)) * st;
#pragma unroll
  for (int n = 0; n < 6; n++) {
    if (n < 5 || cpr == 12) {
      const int cq = (n >= 4 && cpr == 10) ? 16 + pp : 8 * (n >> 1) + 2 * pp + (n & 1);
      u32x2 r0, r1;
      asm volatile("ds_read_b64_tr_b16 %0, %2\n\tds_read_b64_tr_b16 %1, %3\n\ts_waitcnt lgkmcnt(0)"
                   : "=&v"(r0), "=&v"(r1) : "v"(ad + cq * 8), "v"(ad + cq * 8 + 4 * st) : "memory");
      st16(c.rs, dst + n * 1024 + c.lane * 16, (u32x4){r0[0], r0[1], r1[0], r1[1]});
    }
  }
}

constexpr int L_WO = 61440, L_W1 = 20480, L_CS = 143360;
__device__ __forceinline__ void ld16s(u32x4& v, const char* p) { asm volatile("global_load_dwordx4 %0, %1, off sc1" : "=v"(v) : "v"(p)); }
__device__ __forceinline__ void ld_frag(u32x4& v, const Ctx& c, unsigned soff) {
  asm volatile("buffer_load_dwordx4 %0, %1, %2, %3 offen sc1" : "=v"(v) : "v"(c.lane * 16), "s"(c.rd), "s"(soff));
}
__device__ __forceinline__ void ld_sp(u32x2& a, u32x2& b, const char* q) {
  asm volatile("global_load_dwordx2 %0, %2, off sc1\n\tglobal_load_dwordx2 %1, %2, off offset:128 sc1" : "=&v"(a), "=&v"(b) : "v"(q));
}
__device__ __forceinline__ void ld16(u32x4& v, const void* p) { asm volatile("global_load_dwordx4 %0, %1, off" : "=v"(v) : "v"(p)); }
__device__ __forceinline__ void ld8(u32x2& v, const void* p) { asm volatile("global_load_dwordx2 %0, %1, off" : "=v"(v) : "v"(p)); }
template <int N> __device__ __forceinline__ void wait_reg(u32x4& v) { asm volatile("s_waitcnt vmcnt(%1)" : "+v"(v) : "n"(N)); }
__device__ __forceinline__ void raise(Ctx& c, unsigned flag) {
  if (c.tid == 0) __hip_atomic_store((u64*)(c.ws + WS_FLAG) + flag, c.token, __ATOMIC_RELAXED, __HIP_MEMORY_SCOPE_AGENT);
}
#define GATE for (;;)
#define REDO(m) __any(m == ~0u)
__device__ __forceinline__ unsigned nz(unsigned x) { return min(x, 0xfffffffeu); }
__device__ __forceinline__ void st16n(rsrc_t r, unsigned off, u32x4 v) { st16(r, off, (u32x4){nz(v[0]), nz(v[1]), nz(v[2]), nz(v[3])}); }
__device__ __forceinline__ void st8n(rsrc_t r, unsigned off, u32x2 v) { st8(r, off, (u32x2){nz(v[0]), nz(v[1])}); }
__device__ __forceinline__ unsigned umax3(unsigned m, unsigned x, unsigned y) { return max(max(m, x), y); }
__device__ __forceinline__ unsigned seen(unsigned m, const u32x4& v) {
  unsigned r = umax3(umax3(m, v[0], v[1]), v[2], v[3]);
  asm volatile("" : "+v"(r));
  return r;
}
constexpr int kperm(int str, int t) {
  if (str == 6144) { const int f[20] = {2, 4, 7, 9, 12, 14, 17, 19, 0, 1, 3, 5, 6, 8, 10, 11, 13, 15, 16, 18}; return f[t]; }
  return t;
}
__device__ __forceinline__ int kperm2(int t) { return t < 8 ? 3 * t + 2 : (t < 16 ? 3 * t - 24 : 3 * t - 47); }
template <int NT>
__device__ __forceinline__ void redo(Ctx& c, f32x4 (&acc)[NT], unsigned ag, unsigned bg, int nk) {
#pragma unroll
  for (int n = 0; n < NT; n++) acc[n] = (f32x4){0.f, 0.f, 0.f, 0.f};
#pragma unroll 1
  for (int u = 0; u < nk; u++) {
    const int v = u - 8, t = NT == 5 ? kperm2(u) : (u < 8 ? 5 * (u >> 1) + 2 + 2 * (u & 1) : 5 * (v / 3) + (v % 3 == 2 ? 3 : v % 3));
    u32x4 a;
    GATE {
      a = __builtin_amdgcn_raw_buffer_load_b128(c.rs, c.lane * 16, ag + t * 1024, 16);
      if (!__any(seen(0u, a) == ~0u)) break;
    }
#pragma unroll
    for (int n = 0; n < NT; n++) acc[n] = mma16(__builtin_amdgcn_raw_buffer_load_b128(c.rs, c.lane * 16, bg + (t * NT + n) * 1024, 16), a, acc[n]);
  }
}
template <int J, int NT, int STR> __device__ __forceinline__ void rd_b(u32x4& v, const unsigned (&lb)[3]) {
  constexpr int t = J / NT, tt = STR == 10240 ? (t & 15) : kperm(STR, t), per = 61440 / STR;
  asm volatile("ds_read_b128 %0, %1 offset:%2" : "=v"(v) : "v"(lb[tt / per]), "n"((tt % per) * STR + (J % NT) * 1024));
}
template <int OFF> __device__ __forceinline__ void wr_b(unsigned ad, const u32x4& v) {
  asm volatile("ds_write_b128 %0, %1 offset:%2" :: "v"(ad), "v"(v), "n"(OFF) : "memory");
}
template <int N> __device__ __forceinline__ void wait_lds(u32x4& v) { asm volatile("s_waitcnt lgkmcnt(%1)" : "+v"(v) : "n"(N)); }
template <int N> __device__ __forceinline__ void wait_lds2(u32x4& v, u32x4& u) { asm volatile("s_waitcnt lgkmcnt(%2)" : "+v"(v), "+v"(u) : "n"(N)); }
template <int N> __device__ __forceinline__ void wait_lds(u32x2& v) { asm volatile("s_waitcnt lgkmcnt(%1)" : "+v"(v) : "n"(N)); }
template <int T, int N> __device__ __forceinline__ void stair() {
  if constexpr (T == 0) __builtin_amdgcn_s_setprio(3);
  if constexpr (T == N * 3 / 8) __builtin_amdgcn_s_setprio(2);
  if constexpr (T == N * 5 / 8) __builtin_amdgcn_s_setprio(1);
  if constexpr (T == N * 7 / 8) __builtin_amdgcn_s_setprio(0);
}
template <int T0, int T1, int NT, int STR, bool FIRST, int JEND, int NA, int R, class WT>
__device__ __forceinline__ void k_steps(f32x4 (&acc)[NT], u32x4 (&a)[NA], u32x4 (&bf)[R], const unsigned (&lb)[3], WT wt) {
  if constexpr (FIRST) static_for<T0 * NT, T0 * NT + R - 1>([&](auto jc) { rd_b<jc.value, NT, STR>(bf[jc.value % R], lb); });
  wait_lds<R - 2>(bf[T0 * NT % R]);
  static_for<T0 * NT, T1 * NT>([&](auto jc) {
    constexpr int j = jc.value, t = j / NT, n = j % NT, last = (j + R - 1 < JEND ? j + R - 1 : JEND - 1);
    if constexpr (n == 0) wt(std::integral_constant<int, t>{});
    if constexpr (j + R - 1 < JEND) rd_b<j + R - 1, NT, STR>(bf[(j + R - 1) % R], lb);
    if constexpr ((j - T0 * NT) % 2 == 0 && j + 1 < T1 * NT) {
      if constexpr (j + 2 < T1 * NT) wait_lds2<last - j - 2>(bf[(j + 1) % R], bf[(j + 2) % R]);
      else wait_lds<last - j - 1>(bf[(j + 1) % R]);
    }
    acc[n] = mma16(bf[j % R], a[t], acc[n]);
    SB();
  });
#pragma unroll
  for (int n = 0; n < NT; n++) asm volatile("" : "+v"(acc[n]));
}
template <bool WO>
__device__ __forceinline__ void epi80(Ctx& c, const Params& p, f32x4 (&acc)[5], const u32x4 (&e4)[2], const u32x2& e2, u32x2 (&hres)[5], int rb, int cb) {
  const int wm = c.w & 3, g = c.g, r16 = c.r16;
  LAS f32x4* red = (LAS f32x4*)c.lds + wm * 320 + c.lane;
  bar();
  if (c.w >= 4) {
#pragma unroll
    for (int n = 0; n < 5; n++) red[n * 64] = acc[n];
  }
  bar();
  if (c.w < 4) {
    const u32x2 ev[5] = {{e4[0][0], e4[0][1]}, {e4[0][2], e4[0][3]}, {e4[1][0], e4[1][1]}, {e4[1][2], e4[1][3]}, e2};
    u32x2 o[5];
#pragma unroll
    for (int n = 0; n < 5; n++) {
      f32x4 v = acc[n] + red[n * 64];
      v[0] += bflo(ev[n][0]); v[1] += bfhi(ev[n][0]); v[2] += bflo(ev[n][1]); v[3] += bfhi(ev[n][1]);
      if (!WO) { v[0] += bflo(hres[n][0]); v[1] += bfhi(hres[n][0]); v[2] += bflo(hres[n][1]); v[3] += bfhi(hres[n][1]); }
      o[n] = (u32x2){pk2(v[0], v[1]), pk2(v[2], v[3])};
    }
    const int row = rb * 64 + wm * 16 + r16;
    if (WO) {
      const int row16 = row >> 4;
#pragma unroll
      for (int q = 0; q < 2; q++) {
        const int c8 = cb * 10 + q * 4 + g;
        st16n(c.rs, WS_H + ((row16 * 20 + (c8 >> 2)) * 64 + (c8 & 3) * 16 + r16) * 16, (u32x4){o[2 * q][0], o[2 * q][1], o[2 * q + 1][0], o[2 * q + 1][1]});
      }
      const int c8 = cb * 10 + 8 + (g >> 1);
      const auto x0 = __builtin_amdgcn_permlane16_swap(o[4][0], o[4][0], false, false);
      const auto x1 = __builtin_amdgcn_permlane16_swap(o[4][1], o[4][1], false, false);
      if (!(g & 1)) st16n(c.rs, WS_H + ((row16 * 20 + (c8 >> 2)) * 64 + (c8 & 3) * 16 + r16) * 16, (u32x4){x0[0], x1[0], x0[1], x1[1]});
      float s1 = 0.f, s2 = 0.f;
#pragma unroll
      for (int n = 0; n < 5; n++) {
        hres[n] = o[n];
        const float a0 = bflo(o[n][0]), a1 = bfhi(o[n][0]), a2 = bflo(o[n][1]), a3 = bfhi(o[n][1]);
        s1 += (a0 + a1) + (a2 + a3);
        s2 += (a0 * a0 + a1 * a1) + (a2 * a2 + a3 * a3);
      }
      s1 += __shfl_xor(s1, 16); s2 += __shfl_xor(s2, 16);
      s1 += __shfl_xor(s1, 32); s2 += __shfl_xor(s2, 32);
      if (g == 0) st8n(c.rs, WS_SP + row16 * 1024 + cb * 128 + r16 * 8, (u32x2){__builtin_bit_cast(unsigned, s1), __builtin_bit_cast(unsigned, s2)});
    } else {
      bf16* op = p.out + (size_t)row * D + cb * 80;
      *(u32x4*)(op + g * 8) = (u32x4){o[0][0], o[0][1], o[1][0], o[1][1]};
      *(u32x4*)(op + 32 + g * 8) = (u32x4){o[2][0], o[2][1], o[3][0], o[3][1]};
      *(u32x2*)(op + 64 + g * 4) = o[4];
    }
  }
}

__device__ __forceinline__ void tail_phases(Ctx& c, const Params& p, int xcd, int i, u32x4 (&stash)[20]) {
  LAS char* lds = c.lds;
  const int lane = c.lane, w = c.w, r16 = c.r16, g = c.g;
  const int wm = w & 3, wk = w >> 2;
  const int rb64 = xcd * 4 + (i & 3), cb80 = i >> 2, rb128 = xcd * 2 + (i & 1), cb96 = i >> 1;
  const unsigned w1g = WS_W1 + cb96 * 122880, w2g = WS_W2 + cb80 * 245760, l16 = lane * 16;
  auto w2_fetch = [&](int slot) {
    const int ts = slot / 10, r = slot - ts * 10, h = r >= 5;
    blds16(c.rs, l16, w2g + ((h * 24 + kperm2(ts)) * 5 + r - 5 * h) * 1024, lds + slot * 1024);
  };
  const f32x4 z4 = {0.f, 0.f, 0.f, 0.f};
  u32x4 e4[2];
  u32x2 e2, hres[5];
  rawbar();
  if (w < 6) {
#pragma unroll
    for (int q = 0; q < 6; q++) blds16(c.rs, l16, w1g + ((w) * 6 + q) * 1024, lds + L_W1 + (w * 6 + q) * 1024);
  }
  {
    const bf16* xr = p.x + (size_t)(rb64 * 64 + wm * 16 + r16) * D + cb80 * 80;
    ld16(e4[0], xr + g * 8); ld16(e4[1], xr + 32 + g * 8); ld8(e2, xr + 64 + g * 4);
  }
  STAMP(7);
  {
    u32x4 a[10], bf[11];
    const unsigned ag = WS_O + ((rb64 * 4 + wm) * 20 + wk * 10) * 1024;
    GATE {
      unsigned m = 0;
#pragma unroll
      for (int t = 0; t < 10; t++) ld_frag(a[t], c, ag + t * 1024);
#pragma unroll
      for (int t = 0; t < 10; t++) { wait_reg<0>(a[t]); m = seen(m, a[t]); }
      if (!__any(m == ~0u)) break;
    }
    STAMP(8);
    const unsigned l0 = (unsigned)(size_t)lds + L_WO + wk * 51200 + lane * 16, lb[3] = {l0, l0, l0};
    f32x4 acc[5] = {z4, z4, z4, z4, z4};
    k_steps<0, 10, 5, 5120, true, 50>(acc, a, bf, lb, [&](auto) {});
    STAMP(9);
    asm volatile("" : "+v"(e4[0]), "+v"(e4[1]), "+v"(e2));
    epi80<true>(c, p, acc, e4, e2, hres, rb64, cb80);
  }
  if (w >= 4) {
#pragma unroll 1
    for (int t = 2 + w; t < 20; t += 4) {
#pragma unroll
      for (int q = 0; q < 6; q++) blds16(c.rs, l16, w1g + ((t) * 6 + q) * 1024, lds + L_W1 + (t * 6 + q) * 1024);
    }
  }
  STAMP(10);
  if (w >= 4) wait_vm<0>();
  rawbar();
  LAS float* st_cs = (LAS float*)(lds + L_CS);
  if (w < 6) {
    LAS char* bl = lds + L_W1 + lane * 16;
    const u32x4 ones = {0x3f803f80u, 0x3f803f80u, 0x3f803f80u, 0x3f803f80u};
    f32x4 cs0 = z4, cs1 = z4;
#pragma unroll
    for (int t = 0; t < 20; t += 2) {
      cs0 = mma16(ones, *(LAS u32x4*)(bl + (t * 6 + w) * 1024), cs0);
      cs1 = mma16(ones, *(LAS u32x4*)(bl + (t * 6 + 6 + w) * 1024), cs1);
    }
    if (g == 0) st_cs[w * 16 + r16] = cs0[0] + cs1[0];
    asm volatile("s_waitcnt lgkmcnt(0)" ::: "memory");
  }
  rawbar();
  {
    u32x4 a[20], bf[12], bv[3];
    u32x2 sp0, sp1;
    const int row16 = rb128 * 8 + w;
    const unsigned ag = WS_H + row16 * 20480;
#pragma unroll
    for (int j = 0; j < 3; j++) ld16(bv[j], p.b1 + cb96 * 96 + j * 32 + g * 8);
    unsigned m = 0;
    GATE {
      ld_sp(sp0, sp1, c.ws + WS_SP + row16 * 1024 + g * 256 + r16 * 8);
#pragma unroll
      for (int t = 0; t < 8; t++) ld_frag(a[t], c, ag + kperm(6144, t) * 1024);
      asm volatile("s_waitcnt vmcnt(0)" : "+v"(sp0), "+v"(sp1));
      m = umax3(umax3(0u, sp0[0], sp0[1]), sp1[0], sp1[1]);
#pragma unroll
      for (int t = 0; t < 8; t++) { wait_reg<0>(a[t]); m = seen(m, a[t]); }
      if (!__any(m == ~0u)) break;
    }
    m = 0;
    STAMP(11);
#pragma unroll
    for (int t = 8; t < 10; t++) ld_frag(a[t], c, ag + kperm(6144, t) * 1024);
    LAS char* bl = lds + L_W1 + lane * 16;
    f32x4 acc[6] = {z4, z4, z4, z4, z4, z4};
    const unsigned l0 = (unsigned)(size_t)bl, lb[3] = {l0, l0 + 61440, l0};
    asm volatile("s_waitcnt lgkmcnt(0)" ::: "memory");
    k_steps<0, 20, 6, 6144, true, 120>(acc, a, bf, lb, [&](auto tc) {
      constexpr int t = tc.value;
      if constexpr (t >= 1 && t <= 10) ld_frag(a[t + 9], c, ag + kperm(6144, t + 9) * 1024);
      if constexpr (t >= 14 && t < 18) { const int s = w * 4 + t - 14; w2_fetch(s < 20 ? s : (s < 30 ? s + 130 : 159)); }
      if constexpr (t >= 8) { wait_reg<(t < 10 ? 9 : 19 - t) + (t >= 14 ? (t < 18 ? t - 13 : 4) : 0)>(a[t]); m = seen(m, a[t]); }
      stair<t, 20>();
    });
    if (REDO(m)) redo<6>(c, acc, ag, w1g, 20);
    STAMP(12);
    asm volatile("" : "+v"(sp0), "+v"(sp1), "+v"(bv[0]), "+v"(bv[1]), "+v"(bv[2]));
    const f32x2 q2 = __builtin_bit_cast(f32x2, sp0) + __builtin_bit_cast(f32x2, sp1);
    float s1 = q2[0], s2 = q2[1];
    s1 += __shfl_xor(s1, 16); s2 += __shfl_xor(s2, 16);
    s1 += __shfl_xor(s1, 32); s2 += __shfl_xor(s2, 32);
    const float mean = s1 * (1.f / D);
    const float rs = __builtin_amdgcn_rsqf(fmaxf(s2 * (1.f / D) - mean * mean, 0.f) + 1e-5f);
#pragma unroll
    for (int j = 0; j < 3; j++) {
      u32x4 o;
#pragma unroll
      for (int h = 0; h < 2; h++) {
        const int n = 2 * j + h;
        const f32x4 c4 = *(LAS f32x4*)(st_cs + n * 16 + g * 4);
        const float v0 = fmaxf(rs * (acc[n][0] - mean * c4[0]) + bflo(bv[j][2 * h]), 0.f);
        const float v1 = fmaxf(rs * (acc[n][1] - mean * c4[1]) + bfhi(bv[j][2 * h]), 0.f);
        const float v2 = fmaxf(rs * (acc[n][2] - mean * c4[2]) + bflo(bv[j][2 * h + 1]), 0.f);
        const float v3 = fmaxf(rs * (acc[n][3] - mean * c4[3]) + bfhi(bv[j][2 * h + 1]), 0.f);
        o[2 * h] = pk2(v0 * v0, v1 * v1); o[2 * h + 1] = pk2(v2 * v2, v3 * v3);
      }
      st16n(c.rs, WS_G + (row16 * 48 + cb96 * 3 + j) * 1024 + lane * 16, o);
    }
  }
  bar();
  if (w < 5) {
#pragma unroll
    for (int q = 0; q < 10; q++) w2_fetch(20 + w * 10 + q);
  }
  static_for<0, 10>([&](auto qc) { wr_b<qc.value * 1024>((unsigned)(size_t)lds + (70 + w * 10) * 1024 + lane * 16, stash[qc.value]); });
  {
    const bf16* br = p.b2 + cb80 * 80;
    ld16(e4[0], br + g * 8); ld16(e4[1], br + 32 + g * 8); ld8(e2, br + 64 + g * 4);
  }
  STAMP(13);
  wait_vm<0>();
  bar();
  {
    u32x4 a[24], bf[11];
    const unsigned ag = WS_G + ((rb64 * 4 + wm) * 48 + wk * 24) * 1024;
    unsigned m = 0;
    GATE {
      m = 0;
#pragma unroll
      for (int t = 0; t < 8; t++) ld_frag(a[t], c, ag + (3 * t + 2) * 1024);
#pragma unroll
      for (int t = 0; t < 8; t++) { wait_reg<0>(a[t]); m = seen(m, a[t]); }
      if (!__any(m == ~0u)) break;
    }
    m = 0;
    STAMP(14);
#pragma unroll
    for (int t = 8; t < 16; t++) ld_frag(a[t], c, ag + (3 * t - 24) * 1024);
    auto wt = [&](auto tc) {
      constexpr int t = tc.value;
      if constexpr (t >= 1 && t <= 8) ld_frag(a[t + 15], c, ag + (3 * (t + 15) - 47) * 1024);
      if constexpr (t >= 8) { wait_reg<23 - t>(a[t]); m = seen(m, a[t]); }
      stair<t % 8, 8>();
    };
    const unsigned l0 = (unsigned)(size_t)lds + wk * 5120 + lane * 16, lb[3] = {l0, l0 + 61440, l0 + 122880};
    f32x4 acc[5] = {z4, z4, z4, z4, z4};
    k_steps<0, 8, 5, 10240, true, 80>(acc, a, bf, lb, wt);
    rawbar();
    asm volatile("s_waitcnt lgkmcnt(0)" ::: "memory");
    static_for<0, 10>([&](auto qc) { wr_b<qc.value * 1024>((unsigned)(size_t)lds + w * 10240 + lane * 16, stash[10 + qc.value]); });
    k_steps<8, 16, 5, 10240, false, 80>(acc, a, bf, lb, wt);
    rawbar();
    k_steps<16, 24, 5, 10240, true, 120>(acc, a, bf, lb, wt);
    if (REDO(m)) redo<5>(c, acc, ag, w2g + wk * 122880, 24);
    STAMP(15);
    asm volatile("" : "+v"(e4[0]), "+v"(e4[1]), "+v"(e2));
    epi80<false>(c, p, acc, e4, e2, hres, rb64, cb80);
  }
}

__device__ __forceinline__ f32x16 mma32(u32x4 a, u32x4 b, f32x16 c) {
  return __builtin_amdgcn_mfma_f32_32x32x16_bf16(__builtin_bit_cast(bf16x8, a), __builtin_bit_cast(bf16x8, b), c, 0, 0, 0);
}
__device__ __forceinline__ f32x16 mma32h(u32x2 a, u32x2 b, f32x16 c) {
  return __builtin_amdgcn_mfma_f32_32x32x8bf16_1k(__builtin_bit_cast(s16x4, a), __builtin_bit_cast(s16x4, b), c, 0, 0, 0);
}
__device__ __forceinline__ float max16(const f32x16& s) {
  float m = fmaxf(fmaxf(s[0], s[1]), s[2]);
#pragma unroll
  for (int i = 3; i < 15; i += 2) m = fmaxf(fmaxf(m, s[i]), s[i + 1]);
  return fmaxf(m, s[15]);
}
__device__ __forceinline__ u32x2 set_shift(u32x2 qhi, float m, int hh) {
  const float a = bflo(pk2(m, 0.f));
  const float r = m - a;
  const float b = bflo(pk2(r, 0.f));
  const u32x2 e = {pk2(-a, -b), pk2(b - r, 0.f)};
  return hh ? e : qhi;
}
__device__ __forceinline__ u32x2 exp4(const f32x16& s, int i) {
  const float e0 = __builtin_amdgcn_exp2f(s[i]), e1 = __builtin_amdgcn_exp2f(s[i + 1]);
  const float e2 = __builtin_amdgcn_exp2f(s[i + 2]), e3 = __builtin_amdgcn_exp2f(s[i + 3]);
  return (u32x2){pk2(e0, e1), pk2(e2, e3)};
}

template <int OFF> __device__ __forceinline__ void rd8(u32x2& v, unsigned ad) { asm volatile("ds_read_b64 %0, %1 offset:%2" : "=v"(v) : "v"(ad), "n"(OFF)); }
template <int OFF> __device__ __forceinline__ void rd16(u32x4& v, unsigned ad) { asm volatile("ds_read_b128 %0, %1 offset:%2" : "=v"(v) : "v"(ad), "n"(OFF)); }
__device__ __forceinline__ unsigned local_s(unsigned v) { asm volatile("" : "+s"(v)); return v; }
__device__ __forceinline__ void wait_k(u32x2& a, u32x2& b, u32x2& h, u32x4& v) {
  asm volatile("s_waitcnt lgkmcnt(2)" : "+v"(a), "+v"(b), "+v"(h), "+v"(v));
}
template <class T> __device__ __forceinline__ void pin(T& v) { asm volatile("" : "+v"(v)); }
__device__ __forceinline__ void ld_flags(u32x4& a, u32x4& b, const char* fp) {
  asm volatile("global_load_dwordx4 %0, %2, off sc1\n\tglobal_load_dwordx4 %1, %2, off offset:16 sc1" : "=&v"(a), "=&v"(b) : "v"(fp));
}

constexpr int SCHED[] = {300, 0, 1, 106, 107, 2, 3, 301, 4, 5, 100, 101, 6, 7, 302, 8, 9, 102, 103, 311, 10, 11, 303, 12, 13, 104, 105, 310, 14, 15};

__device__ __forceinline__ void attn_phase(Ctx& c, const Params& p, int head, int qb, int i, u32x4 (&stash)[20]) {
  LAS char* lds = c.lds;
  const int lane = c.lane, w = c.w;
  const int r = lane & 31, hh = lane >> 5;
  const int qrow = qb * 256 + w * 32 + r;
  const unsigned kvg = WS_KV + head * KV_HEAD;
  const char* qp = c.ws + WS_Q + (size_t)(head * 5 * S + qrow) * 8;
  u32x2 qa, qc, qhi0;
  asm volatile("global_load_dwordx2 %0, %1, off sc1" : "=v"(qa) : "v"(qp + (size_t)(2 * hh) * S * 8));
  asm volatile("global_load_dwordx2 %0, %1, off sc1" : "=v"(qc) : "v"(qp + (size_t)(2 * hh + 1) * S * 8));
  asm volatile("global_load_dwordx2 %0, %1, off sc1" : "=v"(qhi0) : "v"(qp + (size_t)4 * S * 8));
  auto issue1 = [&](int s, int j) {
    blds16(c.rs, lane * 16, kvg + s * AT_STAGE + (w + 8 * j) * 1024, lds + (s % 3) * AT_STAGE + (w + 8 * j) * 1024);
  };
  const int job = blockIdx.x * 3 + w;
  LAS char* jl = lds + 2 * AT_STAGE + w * 6144;
  issue1(0, 0); issue1(0, 1);
  wait_vm<0>();
  asm volatile("" : "+v"(qa), "+v"(qc), "+v"(qhi0));
  const u32x4 qlo = {qa[0], qa[1], qc[0], qc[1]};
  bar();
  STAMP(5);
  issue1(0, 2); issue1(0, 3); issue1(0, 4); issue1(0, 5);
  if (w < 3) retile_load(c, p, job, jl);

  const int koff = hh * 512 + r * 8, khoff = 1024 + lane * 8, voff = 1536 + hh * 384 + (r <= 20 ? r : 21) * 16;
  const u32x2 qz = hh ? (u32x2){0u, 0u} : qhi0;
  const f32x16 zero = {0.f, 0.f, 0.f, 0.f, 0.f, 0.f, 0.f, 0.f, 0.f, 0.f, 0.f, 0.f, 0.f, 0.f, 0.f, 0.f};
  u32x2 qhi;
  f32x16 sc;
  {
    const u32x2 k0a = *(LAS u32x2*)(lds + koff), k0b = *(LAS u32x2*)(lds + koff + 256);
    const u32x4 k0 = {k0a[0], k0a[1], k0b[0], k0b[1]};
    const u32x2 kh0 = *(LAS u32x2*)(lds + khoff);
    const f32x16 s0 = mma32(k0, qlo, zero);
    float m = max16(mma32h(kh0, qz, s0));
    m = fmaxf(m, __shfl_xor(m, 32));
    qhi = set_shift(qhi0, m, hh);
    sc = mma32h(kh0, qhi, s0);
  }
  f32x16 o = zero;
  const unsigned l0 = (unsigned)(size_t)lds;
  u32x2 ka, kb, khi;
  u32x4 v0, v1;
  rd8<KV_TILE>(ka, l0 + koff); rd8<KV_TILE + 256>(kb, l0 + koff); rd8<KV_TILE>(khi, l0 + khoff);
  rd16<0>(v0, l0 + voff); rd16<768>(v1, l0 + voff);
  u32x4 p1p = {0u, 0u, 0u, 0u}, v1p = p1p, fr0 = p1p, fr1 = p1p;
  const int cb80 = i >> 2;
  unsigned ak[2], ah[2], av[2];
  float el[4] = {0.f, 0.f, 0.f, 0.f};
  auto slot = [&](auto tc, auto lc) {
    constexpr int t = tc.value;
    constexpr bool LAST = lc.value;
    constexpr int xk = t + 2 >= 16, ok = ((t + 2) & 15) * KV_TILE, xv = t + 1 >= 16, ov = ((t + 1) & 15) * KV_TILE;
    if constexpr (t == 14 && !LAST) __builtin_amdgcn_s_setprio(3);
    if constexpr (t == 4) __builtin_amdgcn_s_setprio(2);
    if constexpr (t == 9) __builtin_amdgcn_s_setprio(1);
    if constexpr (t == 12) __builtin_amdgcn_s_setprio(0);
    f32x16 sn;
    float e[16];
    unsigned pp[6];
    static_for<0, sizeof(SCHED) / sizeof(int)>([&](auto oc) {
      constexpr int op = SCHED[oc.value];
      if constexpr (op < 16) { e[op] = __builtin_amdgcn_exp2f(sc[op]); if constexpr (op >= 12) pin(e[op]); }
      else if constexpr (op < 106) { pp[op - 100] = pk2(e[2 * (op - 100)], e[2 * (op - 100) + 1]); pin(pp[op - 100]); }
      else if constexpr (op < 108) p1p[op - 104] = pk2(el[2 * (op - 106)], el[2 * (op - 106) + 1]);
      else if constexpr (op == 300) {
        sn = mma32((u32x4){ka[0], ka[1], kb[0], kb[1]}, qlo, zero);
      } else if constexpr (op == 301) {
        sn = mma32h(khi, qhi, sn);
        SB();
        rd8<ok>(ka, ak[xk]); rd8<ok + 256>(kb, ak[xk]); rd8<ok>(khi, ah[xk]);
      } else if constexpr (op == 302) {
        o = mma32(v1p, p1p, o);
      } else if constexpr (op == 310) {
        wait_k(ka, kb, khi, v1p);
      } else if constexpr (op == 311) {
        wait_lds<4>(v0);
      } else {
        o = mma32(v0, (u32x4){pp[0], pp[1], pp[2], pp[3]}, o);
        SB();
        v1p = v1;
        rd16<ov>(v0, av[xv]); rd16<ov + 768>(v1, av[xv]);
      }
      SB();
    });
    p1p[0] = pp[4]; p1p[1] = pp[5];
    el[0] = e[12]; el[1] = e[13]; el[2] = e[14]; el[3] = e[15];
    sc = sn;
  };
  auto warm = [&](unsigned voff, unsigned soff) {
    __builtin_amdgcn_raw_ptr_buffer_load_lds(c.rs, (LAS void*)(lds + 159744), 4, voff, soff, 0, 16);
  };
  const unsigned w1h = WS_W1 + (i >> 1) * 122880 + (i & 1) * 61440, w2p = WS_W2 + cb80 * 245760;
  const unsigned w2m = w2p + kperm2(7 + w) * 5120, w2l = w2p + kperm2(16 + w) * 5120;
  const int wl = ((i >> 1) & 1) * 140 + (w * 64 + lane < 140 ? w * 64 + lane : 0);
  const unsigned w2v = (3 * (wl / 40) + 2) * 5120 + (wl % 40) * 128;
  const unsigned w1v = w * 8192 + lane * 128 < 61440 ? w * 8192 + lane * 128 : 0;
  __builtin_amdgcn_s_setprio(3);
  wait_k(ka, kb, khi, v1p);
  auto all_flags = [&](unsigned f) {
    wait_reg<0>(fr0); wait_reg<0>(fr1);
    const unsigned lo = (unsigned)c.token, hi = (unsigned)(c.token >> 32);
    if (!__all(fr0[0] == lo && fr0[1] == hi && fr0[2] == lo && fr0[3] == hi && fr1[0] == lo && fr1[1] == hi && fr1[2] == lo && fr1[3] == hi)) {
      spin_flags(c, f + lane, 64, f + 64 + lane, 64);
      spin_flags(c, f + 128 + lane, 64, f + 192 + lane, 64);
    }
  };
#pragma unroll 1
  for (int s = 0; s < 2; s++) {
    const unsigned cb = l0 + s * AT_STAGE, nb = cb + AT_STAGE;
    ak[0] = cb + koff; ak[1] = nb + koff; ah[0] = cb + khoff; ah[1] = nb + khoff; av[0] = cb + voff; av[1] = nb + voff;
    static_for<0, 16>([&](auto tc) {
      constexpr int t = tc.value;
      if constexpr (t < 6) { const unsigned o8 = local_s(s + 1) * AT_STAGE + (w + 8 * t) * 1024; blds16(c.rs, lane * 16, kvg + o8, lds + o8); }
      if constexpr (t == 3) { if (s == 0) { if (w < 3) wait_vm<9>(); else wait_vm<4>(); rawbar(); } }
      if constexpr (t == 4) {
        const unsigned q = local_s(s) * 22 + w, fo = WS_O + blockIdx.x * 45056 + q * 1024 + lane * 16;
        const u32x4 f = {~0u, ~0u, ~0u, ~0u};
        st16(c.rs, fo, f); st16(c.rs, fo + 8192, f);
        if (w < 6) st16(c.rs, fo + 16384, f);
        else if (w == 7 && lane < 32) st16(c.rs, WS_SP + blockIdx.x * 512 + lane * 16, f);
      }
      if constexpr (t == 9) { if (s == 0 && w < 3) { wait_vm<0>(); retile_store(c, p, local_s(job), jl); } }
      if constexpr (t == 9) { if (s == 1 && w == 7) ld_flags(fr0, fr1, c.ws + WS_FLAG + (F_RT + lane * 4) * 8); }
      if constexpr (t == 14) {
        CYC3(23);
        wait_vm<0>();
        if (s == 1 && w == 7) all_flags(F_RT);
        rawbar();
        CYC3(24);
        raise(c, (s == 0 ? F_RT : F_CL) + blockIdx.x);
      }
      slot(tc, std::false_type{});
    });
  }
  ak[0] = ak[1]; ah[0] = ah[1]; av[0] = av[1];
  ak[1] = l0 + koff; ah[1] = l0 + khoff; av[1] = l0 + voff;
  static_for<0, 16>([&](auto tc) {
    constexpr int t = tc.value;
    if constexpr (t < 6) issue1(3, t);
    else {
      constexpr int q0 = 2 * (t - 6), q1 = q0 + 1;
      ld_frag(stash[q0], c, (q0 < 10 ? w2m : w2l) + ((q0 / 5 & 1) * 120 + q0 % 5) * 1024);
      ld_frag(stash[q1], c, (q1 < 10 ? w2m : w2l) + ((q1 / 5 & 1) * 120 + q1 % 5) * 1024);
      if constexpr (t % 2 == 1 && t < 14) { if (w == (t - 7) / 2) warm(w1v, w1h); }
    }
    if constexpr (t == 14) { CYC(27); wait_vm<18>(); rawbar(); CYC(28); }
    SB();
    slot(tc, std::false_type{});
  });
  ak[0] = ak[1]; ah[0] = ah[1]; av[0] = av[1];
  static_for<0, 16>([&](auto tc) {
    constexpr int t = tc.value;
    if constexpr (t == 0) { if (w == 7) ld_flags(fr0, fr1, c.ws + WS_FLAG + (F_CL + lane * 4) * 8); }
    if constexpr (t == 2) { CYC(29); wait_vm<0>(); if (w == 7) all_flags(F_CL); rawbar(); CYC(30); }
    if constexpr (t >= 2 && t < 15) {
      if (t < 14 || w < 4) blds16(c.rs, lane * 16, WS_WO + cb80 * 102400 + (w + 8 * (t - 2)) * 1024, lds + L_WO + (w + 8 * (t - 2)) * 1024);
      if constexpr (t % 3 == 0) { if (w == 3 + t / 3) __builtin_amdgcn_raw_ptr_buffer_load_lds(c.rs, (LAS void*)(lds + AT_STAGE + w * 256), 4, w1v, w1h, 0, 16); }
      if constexpr (t % 3 == 1 && t < 13) { if (w == t / 3 - 1) __builtin_amdgcn_raw_ptr_buffer_load_lds(c.rs, (LAS void*)(lds + AT_STAGE + w * 256), 4, w2v, w2p + (i & 1) * 122880, 0, 16); }
    }
    SB();
    slot(tc, std::true_type{});
  });
  __builtin_amdgcn_s_setprio(0);
  wait_lds<0>(v1p); wait_lds<0>(ka); wait_lds<0>(kb); wait_lds<0>(khi); wait_lds<0>(v0); wait_lds<0>(v1);
  p1p[2] = pk2(el[0], el[1]); p1p[3] = pk2(el[2], el[3]);
  o = mma32(v1p, p1p, o);
  STAMP(6);
  float l = __shfl(o[8], r + 32);
  if (!__all(l < 1.8446744e19f)) {
    const unsigned kb = WS_KV + head * KV_HEAD;
    auto gk = [&](int t) {
      const u32x2 a = __builtin_amdgcn_raw_buffer_load_b64(c.rs, kb + t * KV_TILE + koff, 0, 16);
      const u32x2 b = __builtin_amdgcn_raw_buffer_load_b64(c.rs, kb + t * KV_TILE + koff + 256, 0, 16);
      return mma32((u32x4){a[0], a[1], b[0], b[1]}, qlo, zero);
    };
    float m = -__builtin_inff();
#pragma unroll 1
    for (int t = 0; t < 64; t++)
      m = fmaxf(m, max16(mma32h(__builtin_amdgcn_raw_buffer_load_b64(c.rs, kb + t * KV_TILE + khoff, 0, 16), qz, gk(t))));
    m = fmaxf(m, __shfl_xor(m, 32));
    qhi = set_shift(qhi0, m, hh);
    o = zero;
#pragma unroll 1
    for (int t = 0; t < 64; t++) {
      const f32x16 s2 = mma32h(__builtin_amdgcn_raw_buffer_load_b64(c.rs, kb + t * KV_TILE + khoff, 0, 16), qhi, gk(t));
      const u32x2 pa = exp4(s2, 0), pb = exp4(s2, 4), pc = exp4(s2, 8), pd = exp4(s2, 12);
      o = mma32(__builtin_amdgcn_raw_buffer_load_b128(c.rs, kb + t * KV_TILE + voff, 0, 16), (u32x4){pa[0], pa[1], pb[0], pb[1]}, o);
      o = mma32(__builtin_amdgcn_raw_buffer_load_b128(c.rs, kb + t * KV_TILE + voff + 768, 0, 16), (u32x4){pc[0], pc[1], pd[0], pd[1]}, o);
    }
    l = __shfl(o[8], r + 32);
  }
  const float inv = 1.f / l;
  const auto x0 = __builtin_amdgcn_permlane32_swap(pk2(o[0] * inv, o[1] * inv), pk2(o[4] * inv, o[5] * inv), false, false);
  const auto x1 = __builtin_amdgcn_permlane32_swap(pk2(o[2] * inv, o[3] * inv), pk2(o[6] * inv, o[7] * inv), false, false);
  const int ca = head * DH + (head & 1) * 4 + hh * 8, cr = head * DH + ((head & 1) ? 0 : 16);
  const unsigned ob = WS_O + (qrow >> 4) * 20480 + (qrow & 15) * 16;
  wait_vm<0>();
  st16n(c.rs, ob + (ca >> 3) * 256, (u32x4){x0[0], x1[0], x0[1], x1[1]});
  if (hh == 0) st8n(c.rs, ob + (cr >> 3) * 256 + (cr & 4) * 2, (u32x2){pk2(o[8] * inv, o[9] * inv), pk2(o[10] * inv, o[11] * inv)});
}

__global__ __launch_bounds__(512, 2) void k_block(Params p) {
  extern __shared__ __attribute__((aligned(16))) char smem[];
  Ctx c;
  c.tid = threadIdx.x; c.lane = c.tid & 63; c.w = __builtin_amdgcn_readfirstlane(c.tid >> 6);
  c.r16 = c.lane & 15; c.g = c.lane >> 4;
  c.lds = (LAS char*)smem; c.ws = p.ws;
  c.rs = __builtin_amdgcn_make_buffer_rsrc(p.ws, 0, WS_END, 0x00020000);
  c.rd = (u32x4){(unsigned)(size_t)p.ws, (unsigned)((size_t)p.ws >> 32) & 0xffffu, WS_END, 0x00020000u};
  STAMP(0);
  if (c.w == 7) {
    __builtin_amdgcn_raw_ptr_buffer_load_lds(c.rs, (LAS void*)(c.lds + 150016), 4, (c.lane * 6 + (blockIdx.x >> 3) % 6) * 65536, 0, 0, 0);
    const char* q = c.lane < 20 ? (const char*)p.out + c.lane * 131072 : c.lane < 27 ? (const char*)p.wo + (c.lane - 20) * 131072 : c.lane < 42 ? (const char*)p.w1 + (c.lane - 27) * 131072 : c.lane < 57 ? (const char*)p.w2 + (c.lane - 42) * 131072 : (const char*)p.b1;
    __builtin_amdgcn_global_load_lds((const GAS void*)q, (LAS void*)(c.lds + 150528), 4, 0, 0);
  }
  if (c.w == 6) {
    u64 pc;
    asm volatile("s_getpc_b64 %0" : "=s"(pc));
    const unsigned line = (blockIdx.x >> 3) + 32 * c.lane;
    if (line < 56000 / 128) __builtin_amdgcn_global_load_lds((const GAS void*)((pc & ~127ull) + line * 128), (LAS void*)(c.lds + 151040), 4, 0, 0);
  }
  const int b = blockIdx.x, xcd = b & 7, i = b >> 3;
  {
    const int rb = (xcd & 3) * 4 + (i & 3), cb = (xcd >> 2) * 8 + (i >> 2);
    if (cb < 10) qkv_phase<false>(c, p, rb, cb);
    else if (cb < 15) qkv_phase<true>(c, p, rb, cb);
    else {
      RowLd rl;
      row_load(rl, c, p, rb * 128 + 120 + c.w);
      if (c.w < 6) retile_load(c, p, 768 + rb * 6 + c.w, c.lds + c.w * 6144);
      wait_vm<0>();
      row_landed(rl, c);
      row_stats(rl, c, rb * 128 + 120 + c.w);
      if (c.w < 6) retile_store(c, p, 768 + rb * 6 + c.w, c.lds + c.w * 6144);
    }
    if (cb < 15) publish(c, F_QKV + cb * 16 + rb);
    STAMP(3);
  }
  const int head = xcd * 4 + (i & 3), qb = i >> 2;
  {
    const int c0 = head * 20 / 128, c1 = (head * 20 + 19) / 128, j = c.lane >> 4;
    await(c, F_QKV + (((j & 1) ? c1 : c0) + (j >> 1) * 5) * 16 + c.r16, 64, F_QKV + (((j & 1) ? c1 : c0) + 10) * 16 + c.r16, 32);
  }
  STAMP(4);
  u32x4 stash[20];
  attn_phase(c, p, head, qb, i, stash);
  tail_phases(c, p, xcd, i, stash);
  if (b == 0 && c.tid == 0) __hip_atomic_store((u64*)(p.ws + WS_FLAG) + F_EPOCH, c.epoch + 1, __ATOMIC_RELAXED, __HIP_MEMORY_SCOPE_AGENT);
}

extern "C" void kernel_launch(void* const* d_in, const int* in_sizes, int n_in, void* d_out, int out_size, void* d_ws,
                              size_t ws_size, hipStream_t stream) {
  Params p;
  p.x = (const bf16*)d_in[0]; p.wqkv = (const bf16*)d_in[1]; p.wo = (const bf16*)d_in[2];
  p.w1 = (const bf16*)d_in[4]; p.b1 = (const bf16*)d_in[5]; p.w2 = (const bf16*)d_in[6]; p.b2 = (const bf16*)d_in[7];
  p.out = (bf16*)d_out; p.ws = (char*)d_ws;
  (void)hipFuncSetAttribute((const void*)k_block, hipFuncAttributeMaxDynamicSharedMemorySize, LDS_BYTES);
  k_block<<<256, 512, LDS_BYTES, stream>>>(p);
}
```
